```python
import torch

def _softmax(x, axis=-1):
    return torch.softmax(x, dim=axis)
def _gelu(x):
    return torch.nn.functional.gelu(x, approximate="tanh")
def _silu(x):
    return torch.nn.functional.silu(x)
def _sigmoid(x):
    return torch.sigmoid(x)
def _layernorm(x, axis=-1, eps=1e-5):
    m = x.mean(dim=axis, keepdim=True)
    v = x.var(dim=axis, keepdim=True, unbiased=False)
    return (x - m) / torch.sqrt(v + eps)
def _scatter_add(tgt, idx, src, axis=0):
    return tgt.clone().index_add_(axis, idx, src)
def _segment_sum(src, seg, n):
    out = torch.zeros((n,) + src.shape[1:], dtype=src.dtype)
    return out.index_add_(0, seg, src)
def _pad(x, pw):
    flat = tuple(v for pair in reversed(pw) for v in pair)
    return torch.nn.functional.pad(x, flat)

kernel_name = 'sequential_preLN__sliding_local_dilated__dense_activation_silu__prompt'

def setup_inputs():

    _w = (lambda a: (lambda t: t.to(torch.bfloat16) if t.is_floating_point() else t)(torch.as_tensor(a)))
    def _A_setup():
        B, S, D = 1, 2048, 512
        return {
            "x": _w((lambda *s: torch.randn(*s, dtype=torch.bfloat16) * 0.05)(B, S, D)),
            "wqkv": _w((lambda *s: torch.randn(*s, dtype=torch.bfloat16) * 0.05)(D, 3 * D)),
            "wo": _w((lambda *s: torch.randn(*s, dtype=torch.bfloat16) * 0.05)(D, D)),
        }
    _A_in = _A_setup()

    def _B_setup():
        B, S, D, F = 1, 2048, 512, 1792
        return {
            "x": _w((lambda *s: torch.randn(*s, dtype=torch.bfloat16) * 0.05)(B, S, D)),
            "w1": _w((lambda *s: torch.randn(*s, dtype=torch.bfloat16) * 0.8)(D, F) * (D ** -0.5)),
            "w2": _w((lambda *s: torch.randn(*s, dtype=torch.bfloat16) * 0.8)(F, D) * (F ** -0.5)),
        }
    _B_in = _B_setup()

    return {
        "A_x": _A_in["x"],
        "A_wqkv": _A_in["wqkv"],
        "A_wo": _A_in["wo"],
        "B_x": _B_in["x"],
        "B_w1": _B_in["w1"],
        "B_w2": _B_in["w2"],
    }

def reference(A_x, A_wqkv, A_wo, B_x, B_w1, B_w2):
    def _A_ref(x, wqkv, wo):
        B, S, D = x.shape
        H = 32; Dh = D // H
        W, dil = 8, 2
        qkv = (lambda x, *d: x.permute(*d))((x @ wqkv).reshape(B, S, 3, H, Dh), 2, 0, 3, 1, 4)
        q, k, v = qkv[0], qkv[1], qkv[2]
        scale = Dh ** -0.5
        pos = torch.arange(S)
        off = torch.arange(W) * dil
        idx = pos[:, None] - off[None, :]
        mask = idx >= 0
        idx_c = torch.where(mask, idx, 0).reshape(-1)
        kg = k[:, :, idx_c, :].reshape(B, H, S, W, Dh)
        vg = v[:, :, idx_c, :].reshape(B, H, S, W, Dh)
        logits = torch.einsum("bhsd,bhswd->bhsw", q, kg) * scale
        logits = torch.where(mask[None, None, :, :], logits, -1e9)
        attn = _softmax(logits, axis=-1)
        o = torch.einsum("bhsw,bhswd->bhsd", attn, vg)
        return (lambda x, *d: x.permute(*d))(o, 0, 2, 1, 3).reshape(B, S, D) @ wo

    def _B_ref(x, w1, w2):
        h = torch.einsum("bsd,df->bsf", x, w1)
        h = _silu(h)
        out = torch.einsum("bsf,fd->bsd", h, w2)
        return out

    x = A_x
    h = x + _A_ref(_layernorm(x, axis=-1), A_wqkv, A_wo)
    h2 = h + _B_ref(_layernorm(h, axis=-1), B_w1, B_w2)
    return h2
```

```cpp
#include <hip/hip_runtime.h>
#include <hip/hip_bf16.h>
typedef unsigned short u16;
__device__ __forceinline__ float bf2f(u16 b){ return __uint_as_float(((unsigned)b)<<16); }
__device__ __forceinline__ u16 f2bf(float f){ __hip_bfloat16 h = __float2bfloat16(f); return *(u16*)&h; }
constexpr int S=2048,D=512,F=1792;

__global__ void k_stats(const u16* x, float* mean, float* rstd){
  int r=blockIdx.x; float s=0,q=0;
  for(int i=threadIdx.x;i<D;i+=64){ float v=bf2f(x[r*D+i]); s+=v; q+=v*v; }
  for(int o=32;o;o>>=1){ s+=__shfl_xor(s,o); q+=__shfl_xor(q,o);}
  if(threadIdx.x==0){ float m=s/D; float v=fmaxf(q/D-m*m,0.f); mean[r]=m; rstd[r]=rsqrtf(v+1e-5f);}
}
template<int MODE>
__global__ void k_gemm(const u16* A,const u16* W,u16* C,const u16* res,const float* mean,const float* rstd,int K,int N){
  int n=blockIdx.x*64+threadIdx.x; int r0=blockIdx.y*8;
  float acc[8]={0}; float cs=0;
  for(int k=0;k<K;k++){ float w=bf2f(W[(size_t)k*N+n]); cs+=w;
    for(int i=0;i<8;i++) acc[i]+=bf2f(A[(size_t)(r0+i)*K+k])*w; }
  for(int i=0;i<8;i++){ int r=r0+i; float v;
    if(MODE==0) v=rstd[r]*(acc[i]-mean[r]*cs);
    else if(MODE==1) v=bf2f(res[(size_t)r*N+n])+acc[i];
    else { float p=rstd[r]*(acc[i]-mean[r]*cs); v=p/(1.f+__expf(-p)); }
    C[(size_t)r*N+n]=f2bf(v);}
}
__global__ void k_attn(const u16* qkv,u16* o){
  int t=blockIdx.x, h=threadIdx.x; if(h>=32) return;
  float l[8]; float mx=-3e38f;
  for(int w=0;w<8;w++){ int j=t-2*w; float s;
    if(j<0) s=-1e9f; else { s=0; for(int d=0;d<16;d++) s+=bf2f(qkv[t*1536+h*16+d])*bf2f(qkv[j*1536+512+h*16+d]); s*=0.25f; }
    l[w]=s; mx=fmaxf(mx,s);}
  float sum=0; for(int w=0;w<8;w++){ l[w]=__expf(l[w]-mx); sum+=l[w]; }
  float inv=1.f/sum;
  for(int d=0;d<16;d++){ float a=0; for(int w=0;w<8;w++){ int j=t-2*w; if(j<0) j=0; a+=bf2f(f2bf(l[w]*inv))*bf2f(qkv[j*1536+1024+h*16+d]); }
    o[t*D+h*16+d]=f2bf(a);}
}
extern "C" void kernel_launch(void* const* d_in, const int* in_sizes, int n_in, void* d_out, int out_size, void* d_ws, size_t ws_size, hipStream_t stream){
  const u16* x=(const u16*)d_in[0]; const u16* wqkv=(const u16*)d_in[1]; const u16* wo=(const u16*)d_in[2];
  const u16* w1=(const u16*)d_in[4]; const u16* w2=(const u16*)d_in[5];
  char* ws=(char*)d_ws;
  float* mean=(float*)ws; float* rstd=mean+S;
  u16* qkv=(u16*)(ws+65536); u16* o=qkv+S*1536; u16* h=o+S*D; u16* f=h+S*D;
  k_stats<<<S,64,0,stream>>>(x,mean,rstd);
  k_gemm<0><<<dim3(1536/64,S/8),64,0,stream>>>(x,wqkv,qkv,nullptr,mean,rstd,D,1536);
  k_attn<<<S,64,0,stream>>>(qkv,o);
  k_gemm<1><<<dim3(D/64,S/8),64,0,stream>>>(o,wo,h,x,nullptr,nullptr,D,D);
  k_stats<<<S,64,0,stream>>>(h,mean,rstd);
  k_gemm<2><<<dim3(F/64,S/8),64,0,stream>>>(h,w1,f,nullptr,mean,rstd,D,F);
  k_gemm<1><<<dim3(D/64,S/8),64,0,stream>>>(f,w2,(u16*)d_out,h,nullptr,nullptr,F,D);
}
```

```cpp
#pragma clang diagnostic ignored "-Wint-to-pointer-cast"
#include <hip/hip_runtime.h>
#include <hip/hip_bf16.h>

typedef unsigned u32;
typedef unsigned long long u64;
typedef u32 v4u __attribute__((ext_vector_type(4)));
typedef u32 v2u __attribute__((ext_vector_type(2)));
typedef float v4f __attribute__((ext_vector_type(4)));
typedef float v2f __attribute__((ext_vector_type(2)));
typedef float v16f __attribute__((ext_vector_type(16)));
typedef short v4s __attribute__((ext_vector_type(4)));
typedef __bf16 v8bf __attribute__((ext_vector_type(8)));
typedef __bf16 v2bf __attribute__((ext_vector_type(2)));
#define LAS __attribute__((address_space(3)))
#define GAS __attribute__((address_space(1)))
#define DEV __device__ __forceinline__

#define KSTAMP 0

namespace {
constexpr int SEQ = 2048, DM = 512, FF = 1792;
constexpr u32 O_OFF = 65536, H_OFF = O_OFF + 2097152, F_OFF = H_OFF + 2097152, WS_END = F_OFF + 7340032;
constexpr u32 STAMP_OFF = 16u << 20;
constexpr u32 UNIT = 16384;
constexpr u32 X_OFF = 0, XKC = 10240, WR_OFF = 81920, WSTG = 24576;
constexpr u32 EX_OFF = 6 * UNIT;
constexpr u32 HRES_OFF = 9 * UNIT;
constexpr u32 TOUCH_OFF = 155648;
constexpr u32 CNT_OFF = 155904;
constexpr u32 ST1_OFF = 156160;
constexpr u32 CS_OFF = 160256;
constexpr u32 EPO_OFF = 161024;
constexpr u32 LDS_BYTES = 163840;

DEV v4u mk_rsrc(const void* p, u32 bytes) {
  u64 a = (u64)p;
  v4u r;
  r.x = (u32)a; r.y = (u32)(a >> 32) & 0xffffu; r.z = bytes; r.w = 0x00020000u;
  return r;
}
DEV void dma16(v4u rs, u32 voff, u32 soff, u32 lds) {
  asm volatile("s_mov_b32 m0, %3\n\ts_nop 0\n\tbuffer_load_dwordx4 %0, %1, %2 offen lds" ::"v"(voff), "s"(rs), "s"(soff), "s"(lds) : "memory");
}
DEV void dma16m(v4u rs, u32 voff, u32 soff, u32 lds, u64 mask) {
  asm volatile("s_mov_b32 m0, %3\n\ts_mov_b64 exec, %4\n\tbuffer_load_dwordx4 %0, %1, %2 offen lds\n\ts_mov_b64 exec, -1" ::"v"(voff), "s"(rs), "s"(soff), "s"(lds), "s"(mask) : "memory");
}
DEV void dma4_sc1(v4u rs, u32 voff, u32 soff, u32 lds) {
  asm volatile("s_mov_b32 m0, %3\n\ts_nop 0\n\tbuffer_load_dword %0, %1, %2 offen sc1 lds" ::"v"(voff), "s"(rs), "s"(soff), "s"(lds) : "memory");
}
DEV void dma16x2_sc1(v4u rs, u32 voff, u32 soff, u32 lds) {
  asm volatile("s_mov_b32 m0, %3\n\ts_nop 0\n\tbuffer_load_dwordx4 %0, %1, %2 offen sc1 lds\n\t"
               "buffer_load_dwordx4 %0, %1, %2 offen offset:1024 sc1 lds" ::"v"(voff), "s"(rs), "s"(soff), "s"(lds) : "memory");
}
DEV void dma16x4_sc1(v4u rs, u32 voff, u32 soff, u32 lds) {
  asm volatile("s_mov_b32 m0, %3\n\ts_nop 0\n\tbuffer_load_dwordx4 %0, %1, %2 offen sc1 lds\n\t"
               "buffer_load_dwordx4 %0, %1, %2 offen offset:1024 sc1 lds\n\t"
               "buffer_load_dwordx4 %0, %1, %2 offen offset:2048 sc1 lds\n\t"
               "buffer_load_dwordx4 %0, %1, %2 offen offset:3072 sc1 lds" ::"v"(voff), "s"(rs), "s"(soff), "s"(lds) : "memory");
}
DEV void touch(v4u rs, u32 voff) {
  asm volatile("s_mov_b32 m0, %2\n\ts_nop 0\n\tbuffer_load_dword %0, %1, 0 offen lds" ::"v"(voff), "s"(rs), "s"(TOUCH_OFF) : "memory");
}
template <int N> DEV void wait_vm() { asm volatile("s_waitcnt vmcnt(%0)" ::"n"(N) : "memory"); }
DEV void bar() { asm volatile("s_waitcnt lgkmcnt(0)\n\ts_barrier" ::: "memory"); }
template <int N> DEV void bar_vm() { asm volatile("s_waitcnt vmcnt(%0) lgkmcnt(0)\n\ts_barrier" ::"n"(N) : "memory"); }

DEV v4u lds128(u32 a) { return *(LAS v4u*)a; }
DEV v2u lds64(u32 a) { return *(LAS v2u*)a; }
DEV v4f lds128f(u32 a) { return *(LAS v4f*)a; }
DEV v2f lds64f(u32 a) { return *(LAS v2f*)a; }
DEV float lds32f(u32 a) { return *(LAS float*)a; }
DEV v2u trd(u32 a) { return __builtin_bit_cast(v2u, __builtin_amdgcn_ds_read_tr16_b64_v4i16((LAS v4s*)a)); }
DEV v4u trd2(u32 a0, u32 a1) { v2u x = trd(a0), y = trd(a1); return v4u{x.x, x.y, y.x, y.y}; }
struct LB {
  u32 b[3];
  DEV explicit LB(u32 lanepart) {
#pragma unroll
    for (int i = 0; i < 3; ++i) { b[i] = lanepart + i * 65536u; asm("" : "+v"(b[i])); }
  }
  DEV u32 operator()(u32 off) const { return b[off >> 16] + (off & 0xffffu); }
};
DEV v4f mfma16(v4u a, v4u b, v4f c) {
  return __builtin_amdgcn_mfma_f32_16x16x32_bf16(__builtin_bit_cast(v8bf, a), __builtin_bit_cast(v8bf, b), c, 0, 0, 0);
}
DEV v4f mfma16k16(v2u a, v2u b, v4f c) {
  return __builtin_amdgcn_mfma_f32_16x16x16bf16_1k(__builtin_bit_cast(v4s, a), __builtin_bit_cast(v4s, b), c, 0, 0, 0);
}
DEV v16f mfma32(v4u a, v4u b, v16f c) {
  return __builtin_amdgcn_mfma_f32_32x32x16_bf16(__builtin_bit_cast(v8bf, a), __builtin_bit_cast(v8bf, b), c, 0, 0, 0);
}
DEV float dot2(u32 a, u32 b, float c) {
  return __builtin_amdgcn_fdot2_f32_bf16(__builtin_bit_cast(v2bf, a), __builtin_bit_cast(v2bf, b), c, false);
}
constexpr u32 ONES = 0x3f803f80u;
DEV void stat_acc(v4u f, float& s, float& q) {
  u32 a = f.x, b = f.y, c = f.z, d = f.w;
  s = dot2(a, ONES, s); q = dot2(a, a, q); s = dot2(b, ONES, s); q = dot2(b, b, q);
  s = dot2(c, ONES, s); q = dot2(c, c, q); s = dot2(d, ONES, s); q = dot2(d, d, q);
}
DEV u32 pk(float lo, float hi) { v2bf p = {(__bf16)lo, (__bf16)hi}; return __builtin_bit_cast(u32, p); }
DEV float bflo(u32 x) { return __uint_as_float(x << 16); }
DEV float bfhi(u32 x) { return __uint_as_float(x & 0xffff0000u); }
DEV float add16(float x) { auto r = __builtin_amdgcn_permlane16_swap(__float_as_uint(x), __float_as_uint(x), false, false); return __uint_as_float(r[0]) + __uint_as_float(r[1]); }
DEV float add32(float x) { auto r = __builtin_amdgcn_permlane32_swap(__float_as_uint(x), __float_as_uint(x), false, false); return __uint_as_float(r[0]) + __uint_as_float(r[1]); }
DEV float max16(float x) { auto r = __builtin_amdgcn_permlane16_swap(__float_as_uint(x), __float_as_uint(x), false, false); return fmaxf(__uint_as_float(r[0]), __uint_as_float(r[1])); }
DEV float max32(float x) { auto r = __builtin_amdgcn_permlane32_swap(__float_as_uint(x), __float_as_uint(x), false, false); return fmaxf(__uint_as_float(r[0]), __uint_as_float(r[1])); }
DEV v4u merge16(v2u a, v2u b) {
  auto x = __builtin_amdgcn_permlane16_swap(a.x, b.x, false, false);
  auto y = __builtin_amdgcn_permlane16_swap(a.y, b.y, false, false);
  return v4u{x[0], y[0], x[1], y[1]};
}
#define RLX_AGENT __ATOMIC_RELAXED, __HIP_MEMORY_SCOPE_AGENT
DEV void poll_flags(const GAS u64* f, int n, u64 tag, int lane) {
  const GAS u64* p = f + (lane < n ? lane : 0);
  if (lane < 8) {
    for (int it = 0; it < 40000; ++it) {
      u64 v = __hip_atomic_load(p, RLX_AGENT);
      if (__all(v == tag)) break;
    }
  }
  __builtin_amdgcn_fence(__ATOMIC_ACQUIRE, "wavefront");
}

struct Ctx {
  int lane, w, tt, c, sh, q4;
  v4u rx, rqkv, rwo, rw1, rw2, rws;
};

struct Fr { v4u x0, x1, x2, wq, wk, wv; };
template <int TH>
DEV void p1_loop(const Ctx& C, u32 vW, u32 vWo, v4f (&aK)[3], v4f (&aV)[3], v4f (&aQ)[2], float (&ss)[3], float (&sq)[3], float& csq, v2u (&xres)[2]) {
  const int lane = C.lane, w = C.w, h = w & 3;
  const int tok = lane & 15, g = lane >> 4, q = (lane & 15) >> 2, p = lane & 3;
  const LB xa0(tok * 128 + ((g ^ (tok >> 1)) * 16)), xa1(tok * 128 + (((4 + g) ^ (tok >> 1)) * 16));
  const LB wa((8 * g + q) * 128 + ((h ^ ((q >> 1) | ((g & 1) << 1))) * 32) + 8 * p);
  auto open_stage = [&](int s) {
    if (s == 0) { if (w < 6) wait_vm<11>(); bar(); }
    else if (s == 1) { if (w < 6) wait_vm<5>(); bar(); }
    else if (s == 2) { if (w < 6) wait_vm<3>(); bar(); }
    else if (s <= 5) bar_vm<3>();
    else if (s == 6) bar_vm<4>();
    else bar_vm<1>();
  };
  auto refill = [&](int s) {
    if (s >= 1 && s <= 5) {
      const int sn = s + 2;
      const u32 so = (u32)(64 * sn + 8 * w) * 3072u + (u32)C.c * 128u;
      const u32 ld = WR_OFF + (sn % 3) * WSTG + (u32)w * 1024u;
#pragma unroll
      for (int m = 0; m < 3; ++m) dma16(C.rqkv, vW, so + m * 1024u, ld + m * 8192u);
    }
    if (s == 5) {
      if (TH == 0) {
        const u32 L = (u32)(w * 32 + (lane >> 1)) * 8 + C.sh;
        touch(C.rw1, (L >> 2) * 3584u + (u32)C.c * 512u + (L & 3) * 128u + (lane & 1) * 64u);
      } else {
        const u32 L = (u32)((w - 4) * 16 + (lane >> 2)) * 8 + C.sh;
        touch(C.rwo, L * 1024u + (u32)C.c * 128u + (lane & 3) * 32u);
      }
    }
    if (s == 7) {
#pragma unroll
      for (int i = 0; i < 8; ++i) dma16(C.rwo, vWo, (u32)(w + 8 * i) * 8192u + (u32)C.c * 128u, (u32)(w + 8 * i) * 1024u);
    }
    if (s == 4) {
#pragma unroll
      for (int a = 0; a < 2; ++a)
        xres[a] = lds64(X_OFF + (u32)C.c * XKC + (16 + 32 * TH + 16 * a + tok) * 128 + (((2 * h + (g >> 1)) ^ (tok >> 1)) * 16) + (g & 1) * 8);
    }
  };
  auto rd = [&](int k) {
    const int s = k >> 1, ks = k & 1;
    const LB& xa = ks ? xa1 : xa0;
    const u32 xb = X_OFF + s * XKC + 2 * TH * 2048, wb = WR_OFF + (s % 3) * WSTG + ks * 4096;
    Fr f;
    f.wk = trd2(wa(wb + 8192), wa(wb + 8192 + 512));
    f.x0 = lds128(xa(xb)); f.x1 = lds128(xa(xb + 2048)); f.x2 = lds128(xa(xb + 4096));
    f.wv = trd2(wa(wb + 16384), wa(wb + 16384 + 512));
    f.wq = trd2(wa(wb), wa(wb + 512));
    return f;
  };
  open_stage(0);
  Fr f = rd(0);
#pragma unroll
  for (int k = 0; k < 16; ++k) {
    Fr n;
    if (k < 15) {
      if (k & 1) open_stage((k + 1) >> 1);
      n = rd(k + 1);
      if (k & 1) refill((k + 1) >> 1);
    }
    __builtin_amdgcn_sched_barrier(0);
    if (TH == 0) aK[0] = mfma16(f.wk, f.x0, aK[0]);
    aK[1] = mfma16(f.wk, f.x1, aK[1]);
    aK[2] = mfma16(f.wk, f.x2, aK[2]);
    aV[0] = mfma16(f.x0, f.wv, aV[0]);
    aV[1] = mfma16(f.x1, f.wv, aV[1]);
    if (TH == 1) aV[2] = mfma16(f.x2, f.wv, aV[2]);
    aQ[0] = mfma16(f.wq, f.x1, aQ[0]);
    aQ[1] = mfma16(f.wq, f.x2, aQ[1]);
    if (TH == 1) { u32 a = f.wq.x, b = f.wq.y, c2 = f.wq.z, d = f.wq.w; csq = dot2(a, ONES, csq); csq = dot2(b, ONES, csq); csq = dot2(c2, ONES, csq); csq = dot2(d, ONES, csq); }
    if ((k & 3) == h) {
      if (TH == 0) stat_acc(f.x0, ss[0], sq[0]);
      stat_acc(f.x1, ss[1], sq[1]);
      stat_acc(f.x2, ss[2], sq[2]);
    }
    __builtin_amdgcn_sched_barrier(0);
    if (k < 15) f = n;
  }
}

DEV void dma_w1(const Ctx& C, u32 vW1, int u, int unit) {
#pragma unroll
  for (int i = 0; i < 2; ++i) {
    const int n = C.w + 8 * i;
    dma16(C.rw1, vW1, (u32)(32 * u + 2 * n) * 3584u + (u32)C.c * 512u, (u32)unit * UNIT + (u32)n * 1024u);
  }
}
DEV void dma_w2(const Ctx& C, u32 vW2, int v, int unit) {
#pragma unroll
  for (int i = 0; i < 2; ++i) {
    const int n = 2 * C.w + i;
    dma16(C.rw2, vW2, (u32)(448 * (n >> 2) + 32 * v + 8 * (n & 3)) * 1024u + (u32)C.c * 128u, (u32)unit * UNIT + (u32)n * 1024u);
  }
}
DEV void dma_f(const Ctx& C, int a, int j, int unit) {
  const int kq = C.w >> 1, fi = C.w & 1;
  dma16x2_sc1(C.rws, (u32)C.lane * 16u, F_OFF + (u32)((C.tt * 2 + a) * 112 + 28 * kq + 4 * j + 2 * fi) * 1024u,
              (u32)unit * UNIT + (u32)(4 * kq + 2 * fi) * 1024u);
}
}

__global__ void __launch_bounds__(512, 2)
fused_block(const void* __restrict__ x, const void* __restrict__ wqkv, const void* __restrict__ wo, const void* __restrict__ w1,
            const void* __restrict__ w2, void* __restrict__ out, void* __restrict__ ws) {
  extern __shared__ __attribute__((aligned(16))) char smem[];
#define STAMP(i) do {} while (0)
  STAMP(0);
  Ctx C;
  const int tid = threadIdx.x, lane = tid & 63, w = __builtin_amdgcn_readfirstlane(tid >> 6);
  const int b = blockIdx.x, xcd = b & 7, idx = b >> 3;
  const int tt = 8 * (xcd >> 1) + (idx >> 2), c = 4 * (xcd & 1) + (idx & 3);
  C.lane = lane; C.w = w; C.tt = tt; C.c = c; C.sh = idx >> 2; C.q4 = idx & 3;
  C.rx = mk_rsrc(x, SEQ * DM * 2); C.rqkv = mk_rsrc(wqkv, DM * 3 * DM * 2); C.rwo = mk_rsrc(wo, DM * DM * 2);
  C.rw1 = mk_rsrc(w1, c < 7 ? DM * FF * 2 : 0); C.rw2 = mk_rsrc(w2, FF * DM * 2); C.rws = mk_rsrc(ws, WS_END);
  const __amdgpu_buffer_rsrc_t rsW = __builtin_amdgcn_make_buffer_rsrc(ws, 0, WS_END, 0x00020000);
  const __amdgpu_buffer_rsrc_t rsF = __builtin_amdgcn_make_buffer_rsrc(ws, 0, c < 7 ? WS_END : 0, 0x00020000);
  GAS u64* ctrl = (GAS u64*)ws + tt * 128;
  const int tok = lane & 15, g = lane >> 4;
  const int th = w >> 2, h = w & 3;
  const int t0 = 64 * tt;

  const u32 par = w & 1;
  const u32 vX = (lane >> 3) * 1024u + (((lane & 7) ^ (4 * par + (lane >> 4))) * 16u);
  const u32 chW = ((((lane & 7) >> 1) ^ (((lane >> 4) & 1) | (par << 1))) * 2 + (lane & 1)) * 16u;
  const u32 vW = (lane >> 3) * 3072u + chW;
  const u32 vWo = (lane >> 3) * 1024u + chW;
  auto dma_x = [&](int n) {
    const int kc = n / 10, rb = n - 10 * kc;
    int tb = t0 - 16 + 8 * rb; tb = tb < 0 ? 0 : tb;
    const u32 so = (u32)tb * 1024u + (u32)kc * 128u, ld = X_OFF + (u32)kc * XKC + (u32)rb * 1024u;
    dma16m(C.rx, vX, so, ld, rb == 0 ? ~0xffull : ~0ull);
  };
  auto dma_w0 = [&](int s) {
#pragma unroll
    for (int i = 0; i < 4; ++i) {
      const int n = w + 6 * i, m = n >> 3, rb = n & 7;
      dma16(C.rqkv, vW, (u32)(64 * s + 8 * rb) * 3072u + (u32)m * 1024u + (u32)c * 128u, WR_OFF + s * WSTG + (u32)n * 1024u);
    }
  };
  if (w < 6) {
    dma_x(w); dma_x(w + 6); dma_w0(0); dma_x(w + 12); dma_x(w + 18); dma_w0(1); dma_x(w + 24); dma_w0(2);
  } else {
    if (w == 6) {
#pragma unroll
      for (int m = 0; m < 3; ++m) touch(C.rqkv, (u32)(lane * 8 + C.sh) * 3072u + m * 1024u + (u32)c * 128u);
      *(LAS v4u*)(X_OFF + (lane >> 3) * XKC + (lane & 7) * 16) = v4u{ONES, ONES, ONES, ONES};
      if (lane == 0) *(LAS u32*)(CNT_OFF) = 0;
    } else {
#pragma unroll
      for (int m = 0; m < 3; ++m) {
        int id = m * 64 + lane; id = id < 160 ? id : 159;
        const int L = id * 4 + C.q4, kc = L / 80, row = L - 80 * kc;
        int tk = t0 - 16 + row; tk = tk < 0 ? 0 : tk;
        touch(C.rx, (u32)tk * 1024u + (u32)kc * 128u);
      }
      dma4_sc1(C.rws, (lane & 1) * 4u, (u32)tt * 1024u, EPO_OFF);
    }
    for (int i = 0; i < 25; ++i) dma_x(30 + (w - 6) + 2 * i);
  }
  v4f aK[3], aV[3], aQ[2];
#pragma unroll
  for (int i = 0; i < 3; ++i) { aK[i] = v4f{0, 0, 0, 0}; aV[i] = v4f{0, 0, 0, 0}; }
  aQ[0] = v4f{0, 0, 0, 0}; aQ[1] = v4f{0, 0, 0, 0};
  float ss[3] = {0, 0, 0}, sq[3] = {0, 0, 0}, csq = 0;
  v2u xres[2];
  STAMP(1);
  if (th == 0) p1_loop<0>(C, vW, vWo, aK, aV, aQ, ss, sq, csq, xres);
  else p1_loop<1>(C, vW, vWo, aK, aV, aQ, ss, sq, csq, xres);
  STAMP(2);
  {
#pragma unroll
    for (int i = 0; i < 3; ++i) { ss[i] = add32(add16(ss[i])); sq[i] = add32(add16(sq[i])); }
    csq = add32(add16(csq));
    if (th == 0) {
      *(LAS v4f*)(EX_OFF + (h * 2 + 0) * 1024 + lane * 16) = aK[2];
      if (tok == 0) *(LAS v4f*)(CS_OFF + (h * 3 + 1) * 64 + g * 16) = aK[0];
      if (lane < 16) {
        *(LAS float*)(CS_OFF + (h * 3 + 2) * 64 + lane * 4) = aV[0].x;
#pragma unroll
        for (int i = 0; i < 3; ++i) *(LAS v2f*)(ST1_OFF + (h * 80 + 16 * i + lane) * 8) = v2f{ss[i], sq[i]};
      }
    } else {
      *(LAS v4f*)(EX_OFF + (h * 2 + 1) * 1024 + lane * 16) = aV[0];
      if (lane < 16) {
        *(LAS float*)(CS_OFF + (h * 3 + 0) * 64 + lane * 4) = csq;
#pragma unroll
        for (int i = 1; i < 3; ++i) *(LAS v2f*)(ST1_OFF + (h * 80 + 32 + 16 * i + lane) * 8) = v2f{ss[i], sq[i]};
      }
    }
  }
  bar();
  const u32 vW1 = (lane >> 5) * 3584u + (((((lane & 31) >> 2) ^ (2 * par + (lane >> 5))) * 4 + (lane & 3)) * 16u);
  dma_w1(C, vW1, 0, 4); dma_w1(C, vW1, 1, 5); dma_w1(C, vW1, 2, 7); dma_w1(C, vW1, 3, 8);
  {
    v4f ex = lds128f(EX_OFF + (h * 2 + (1 - th)) * 1024 + lane * 16);
    if (th == 0) aV[2] = ex; else aK[0] = ex;
    const v4f cq = lds128f(CS_OFF + (h * 3 + 0) * 64 + g * 16), ck = lds128f(CS_OFF + (h * 3 + 1) * 64 + g * 16);
    const float cv = lds32f(CS_OFF + (h * 3 + 2) * 64 + tok * 4);
    v2u Kp[3], Vp[3], Qp[2];
    const u32 pr = EX_OFF + 8192 + w * 512;
    float rs[3], mr[3];
#pragma unroll
    for (int i = 0; i < 3; ++i) {
      v2f t = lds64f(ST1_OFF + (32 * th + 16 * i + tok) * 8);
#pragma unroll
      for (int hh = 1; hh < 4; ++hh) t += lds64f(ST1_OFF + (hh * 80 + 32 * th + 16 * i + tok) * 8);
      const float mean = t.x * (1.f / DM);
      const float var = fmaxf(t.y * (1.f / DM) - mean * mean, 0.f);
      rs[i] = __builtin_amdgcn_rsqf(var + 1e-5f); mr[i] = mean * rs[i];
      if (lane < 16) { *(LAS float*)(pr + i * 128 + lane * 4) = rs[i]; *(LAS float*)(pr + i * 128 + 64 + lane * 4) = mr[i]; }
    }
#pragma unroll
    for (int i = 0; i < 3; ++i) {
      v4f k = aK[i] * rs[i] - ck * mr[i];
      Kp[i] = v2u{pk(k.x, k.y), pk(k.z, k.w)};
      const v4f r4 = lds128f(pr + i * 128 + g * 16), m4 = lds128f(pr + i * 128 + 64 + g * 16);
      v4f v = aV[i] * r4 - m4 * cv;
      Vp[i] = v2u{pk(v.x, v.y), pk(v.z, v.w)};
      if (i > 0) { v4f qv = aQ[i - 1] * rs[i] - cq * mr[i]; Qp[i - 1] = v2u{pk(qv.x, qv.y), pk(qv.z, qv.w)}; }
    }
    const bool odd = tok & 1;
    const int d0 = tok - 4 * g - (tok & 1);
    const bool cur0 = d0 >= 0, cur1 = d0 - 2 >= 0;
    v2u Op[2];
#pragma unroll
    for (int i = 0; i < 2; ++i) {
      const v4f z = {0, 0, 0, 0};
      const v4f sp = mfma16k16(Kp[i], Qp[i], z), sc = mfma16k16(Kp[i + 1], Qp[i], z);
      float p0 = (odd ? sp.y : sp.x) * 0.25f, p1 = (odd ? sp.w : sp.z) * 0.25f;
      if (i == 0 && th == 0 && tt == 0) { p0 = -1e9f; p1 = -1e9f; }
      const float c0 = (odd ? sc.y : sc.x) * 0.25f, c1 = (odd ? sc.w : sc.z) * 0.25f;
      const float s0 = cur0 ? c0 : p0, s1 = cur1 ? c1 : p1;
      const float mx = max32(max16(fmaxf(s0, s1)));
      const float e0 = __builtin_amdgcn_exp2f((s0 - mx) * 1.44269504f), e1 = __builtin_amdgcn_exp2f((s1 - mx) * 1.44269504f);
      const float inv = __builtin_amdgcn_rcpf(add32(add16(e0 + e1)));
      const u32 w0 = odd ? pk(0.f, e0 * inv) : pk(e0 * inv, 0.f), w1v = odd ? pk(0.f, e1 * inv) : pk(e1 * inv, 0.f);
      const v4u P = {cur0 ? 0u : w0, cur1 ? 0u : w1v, cur0 ? w0 : 0u, cur1 ? w1v : 0u};
      const v4u Vf = {Vp[i].x, Vp[i].y, Vp[i + 1].x, Vp[i + 1].y};
      const v4f o = mfma16(Vf, P, z);
      Op[i] = v2u{pk(o.x, o.y), pk(o.z, o.w)};
    }
    const v4u o16 = merge16(Op[0], Op[1]);
    const u32 off = O_OFF + (u32)((tt * 4 + 2 * th + (g & 1)) * 16 + 2 * c + (h >> 1)) * 1024u + (u32)((2 * (h & 1) + (g >> 1)) * 16 + tok) * 16u;
    __builtin_amdgcn_raw_buffer_store_b128(o16, rsW, off, 0, 16);
  }
  STAMP(3);
  bar_vm<0>();
  u64 tag;
  {
    const v2u e = lds64(EPO_OFF);
    const u64 ep = ((u64)(u32)__builtin_amdgcn_readfirstlane(e.y) << 32) | (u32)__builtin_amdgcn_readfirstlane(e.x);
    tag = ep * 0x9E3779B97F4A7C15ull + 0x632BE59BD9B4E019ull;
  }
  if (w == 7 && lane == 0) __hip_atomic_store(ctrl + 16 + c, tag, RLX_AGENT);
  STAMP(4);
  if (w < 4) {
    const u32 L = (u32)(128 + w * 32 + (lane >> 1)) * 8 + C.sh;
    touch(C.rw1, (L >> 2) * 3584u + (u32)c * 512u + (L & 3) * 128u + (lane & 1) * 64u);
  }
  dma_w1(C, vW1, 4, 6);
  v4u wof[16], w1f[32];
  v16f cs;
#pragma unroll
  for (int i = 0; i < 16; ++i) cs[i] = 0.f;
  const v4u ones4 = {ONES, ONES, ONES, ONES};
  const int q = (lane & 15) >> 2, p = lane & 3, hf = lane >> 5, blk = (lane >> 4) & 1;
  const LB w1a((8 * hf + q) * 512 + ((w ^ q) * 64) + 32 * blk + 8 * p);
  const LB lin(lane * 16);
#define RD_W1(u, unit) do { w1f[2 * (u)] = trd2(w1a((unit) * UNIT), w1a((unit) * UNIT + 2048)); \
                            w1f[2 * (u) + 1] = trd2(w1a((unit) * UNIT + 8192), w1a((unit) * UNIT + 8192 + 2048)); } while (0)
#define CS_W1(u) do { cs = mfma32(w1f[2 * (u)], ones4, cs); cs = mfma32(w1f[2 * (u) + 1], ones4, cs); } while (0)
  {
    const u32 wa = (8 * g + q) * 128 + ((h ^ ((q >> 1) | ((g & 1) << 1))) * 32) + 8 * p;
#pragma unroll
    for (int ks = 0; ks < 16; ++ks) wof[ks] = trd2(wa + ks * 4096, wa + ks * 4096 + 512);
  }
  RD_W1(0, 4); RD_W1(1, 5); RD_W1(2, 7); RD_W1(3, 8);
  bar();
  dma_w1(C, vW1, 5, 4); dma_w1(C, vW1, 6, 5);
  CS_W1(0); CS_W1(1); CS_W1(2); CS_W1(3);
  if (w == 7) poll_flags(ctrl + 16, 8, tag, lane);
  bar();
  STAMP(5);
  dma16x4_sc1(C.rws, lane * 16u, O_OFF + (u32)tt * 65536u + (u32)w * 8192u, (u32)w * 8192u);
  dma16x4_sc1(C.rws, lane * 16u, O_OFF + (u32)tt * 65536u + (u32)w * 8192u + 4096u, (u32)w * 8192u + 4096u);
  dma_w1(C, vW1, 7, 7); dma_w1(C, vW1, 8, 8);
  bar_vm<12>();
  RD_W1(4, 6); RD_W1(5, 4); RD_W1(6, 5);
  bar_vm<4>();
  STAMP(6);
  dma_w1(C, vW1, 9, 4); dma_w1(C, vW1, 10, 5); dma_w1(C, vW1, 11, 6);
  {
    v4f a0 = {0, 0, 0, 0}, a1 = {0, 0, 0, 0};
    const u32 oa = (u32)(2 * th) * 16384u + lane * 16u;
#pragma unroll
    for (int ks = 0; ks < 16; ++ks) {
      a0 = mfma16(wof[ks], lds128(oa + ks * 1024), a0);
      a1 = mfma16(wof[ks], lds128(oa + 16384 + ks * 1024), a1);
    }
    const v2u h0 = {pk(bflo(xres[0].x) + a0.x, bfhi(xres[0].x) + a0.y), pk(bflo(xres[0].y) + a0.z, bfhi(xres[0].y) + a0.w)};
    const v2u h1 = {pk(bflo(xres[1].x) + a1.x, bfhi(xres[1].x) + a1.y), pk(bflo(xres[1].y) + a1.z, bfhi(xres[1].y) + a1.w)};
    const v4u h16 = merge16(h0, h1);
    __builtin_amdgcn_raw_buffer_store_b128(h16, rsW, H_OFF + (u32)((tt * 2 + th) * 32 + 4 * c + h) * 1024u + lane * 16u, 0, 16);
    *(LAS v4u*)(HRES_OFF + w * 1024 + lane * 16) = h16;
  }
  STAMP(7);
  bar_vm<0>();
  if (w == 7) { if (lane == 0) __hip_atomic_store(ctrl + 32 + c, tag, RLX_AGENT); }
  else {
    const u32 L = (u32)(w * 32 + (lane >> 1)) * 8 + C.sh;
    touch(C.rw2, L * 1024u + (u32)c * 128u + (lane & 1) * 64u);
  }
  STAMP(8);
  dma_w1(C, vW1, 12, 0); dma_w1(C, vW1, 13, 1); dma_w1(C, vW1, 14, 2); dma_w1(C, vW1, 15, 3);
  RD_W1(7, 7); RD_W1(8, 8); RD_W1(9, 4); RD_W1(10, 5); RD_W1(11, 6);
  CS_W1(4); CS_W1(5); CS_W1(6); CS_W1(7); CS_W1(8); CS_W1(9); CS_W1(10); CS_W1(11);
  if (w == 7) poll_flags(ctrl + 32, 8, tag, lane);
  bar();
  STAMP(9);
  constexpr u32 HT = 5 * UNIT;
  dma16x4_sc1(C.rws, lane * 16u, H_OFF + (u32)tt * 65536u + (u32)w * 4096u, HT + (u32)w * 4096u);
  dma16x4_sc1(C.rws, lane * 16u, H_OFF + (u32)tt * 65536u + 32768u + (u32)w * 4096u, HT + 32768u + (u32)w * 4096u);
  const u32 vW2 = (lane >> 3) * 1024u + (((lane & 7) ^ (((lane >> 4) & 1) << 2)) * 16u);
  dma_w2(C, vW2, 0, 4);
  bar_vm<10>();
  RD_W1(12, 0); RD_W1(13, 1); RD_W1(14, 2); RD_W1(15, 3);
  CS_W1(12); CS_W1(13); CS_W1(14); CS_W1(15);
  bar_vm<2>();
  STAMP(10);
  dma_w2(C, vW2, 1, 0); dma_w2(C, vW2, 2, 1); dma_w2(C, vW2, 3, 2); dma_w2(C, vW2, 4, 3);
  {
#pragma unroll
    for (int a = 0; a < 2; ++a) {
      float s = 0, qq = 0;
#pragma unroll
      for (int i = 0; i < 4; ++i) stat_acc(lds128(HT + a * 32768 + (4 * w + i) * 1024 + lane * 16), s, qq);
      s = add32(s); qq = add32(qq);
      if (lane < 32) *(LAS v2f*)(ST1_OFF + (w * 64 + a * 32 + lane) * 8) = v2f{s, qq};
    }
  }
  bar();
  auto p3_epi = [&](int a, const v16f& acc) {
    v2f t = lds64f(ST1_OFF + (a * 32 + (lane & 31)) * 8);
#pragma unroll
    for (int ww = 1; ww < 8; ++ww) t += lds64f(ST1_OFF + (ww * 64 + a * 32 + (lane & 31)) * 8);
    const float mean = t.x * (1.f / DM);
    const float var = fmaxf(t.y * (1.f / DM) - mean * mean, 0.f);
    const float rs = __builtin_amdgcn_rsqf(var + 1e-5f), mr = mean * rs;
    u32 o[8];
#pragma unroll
    for (int i = 0; i < 8; ++i) {
      float y[2];
#pragma unroll
      for (int e = 0; e < 2; ++e) {
        const float v = acc[2 * i + e] * rs - cs[2 * i + e] * mr;
        y[e] = v * __builtin_amdgcn_rcpf(1.f + __builtin_amdgcn_exp2f(v * -1.44269504f));
      }
      o[i] = pk(y[0], y[1]);
    }
    const u32 off = F_OFF + (u32)((tt * 2 + a) * 112 + 16 * c + 2 * w) * 1024u + lane * 16u;
    __builtin_amdgcn_raw_buffer_store_b128(v4u{o[0], o[1], o[2], o[3]}, rsF, off, 0, 16);
    __builtin_amdgcn_raw_buffer_store_b128(v4u{o[4], o[5], o[6], o[7]}, rsF, off + 1024u, 0, 16);
  };
  v16f accB;
  {
    v16f acc;
#pragma unroll
    for (int i = 0; i < 16; ++i) { acc[i] = 0.f; accB[i] = 0.f; }
#pragma unroll
    for (int s = 0; s < 32; ++s) acc = mfma32(w1f[s], lds128(lin(HT + s * 1024)), acc);
    p3_epi(0, acc);
#pragma unroll
    for (int s = 0; s < 20; ++s) accB = mfma32(w1f[s], lds128(lin(HT + 32768 + s * 1024)), accB);
    wait_vm<0>();
    {
      u32 old = 0;
      if (lane == 0) old = __hip_atomic_fetch_add((LAS u32*)CNT_OFF, 1u, __ATOMIC_RELAXED, __HIP_MEMORY_SCOPE_WORKGROUP);
      if (lane == 0 && old == 7) __hip_atomic_store(ctrl + 48 + c, tag, RLX_AGENT);
    }
#pragma unroll
    for (int s = 20; s < 32; ++s) accB = mfma32(w1f[s], lds128(lin(HT + 32768 + s * 1024)), accB);
  }
  bar();
  STAMP(11);
  dma_w2(C, vW2, 5, 5); dma_w2(C, vW2, 6, 6); dma_w2(C, vW2, 7, 7); dma_w2(C, vW2, 8, 8);
  p3_epi(1, accB);
  v4u w2f[28];
  const int kq = w >> 1, fi = w & 1;
  const LB w2a(kq * 4096 + (4 * hf + q) * 128 + ((fi ^ (q >> 1)) * 64) + 32 * blk + 8 * p);
#define RD_W2(v, unit) do { w2f[2 * (v)] = trd2(w2a((unit) * UNIT), w2a((unit) * UNIT + 1024)); \
                            w2f[2 * (v) + 1] = trd2(w2a((unit) * UNIT + 2048), w2a((unit) * UNIT + 2048 + 1024)); } while (0)
  RD_W2(0, 4); RD_W2(1, 0); RD_W2(2, 1); RD_W2(3, 2); RD_W2(4, 3);
  bar_vm<0>();
  if (w == 7 && lane == 0) __hip_atomic_store(ctrl + 64 + c, tag, RLX_AGENT);
  STAMP(12);
  dma_w2(C, vW2, 9, 0); dma_w2(C, vW2, 10, 1); dma_w2(C, vW2, 11, 2); dma_w2(C, vW2, 12, 3); dma_w2(C, vW2, 13, 4);
  RD_W2(5, 5); RD_W2(6, 6); RD_W2(7, 7); RD_W2(8, 8);
  if (w == 7) poll_flags(ctrl + 48, 7, tag, lane);
  bar();
  STAMP(13);
  dma_f(C, 0, 0, 5); dma_f(C, 0, 1, 6); dma_f(C, 0, 2, 7); dma_f(C, 0, 3, 8);
  bar_vm<8>();
  RD_W2(9, 0); RD_W2(10, 1); RD_W2(11, 2); RD_W2(12, 3); RD_W2(13, 4);
  bar_vm<0>();
  dma_f(C, 0, 4, 0); dma_f(C, 0, 5, 1); dma_f(C, 0, 6, 2);
  const LB fa(kq * 4096 + lane * 16);
  v16f oA, oB;
#pragma unroll
  for (int i = 0; i < 16; ++i) { oA[i] = 0.f; oB[i] = 0.f; }
#define P4_STAGE(acc, j, unit) do { _Pragma("unroll") for (int i = 0; i < 4; ++i) acc = mfma32(w2f[4 * (j) + i], lds128(fa((unit) * UNIT + i * 1024)), acc); } while (0)
  P4_STAGE(oA, 0, 5); P4_STAGE(oA, 1, 6); P4_STAGE(oA, 2, 7); P4_STAGE(oA, 3, 8);
  if (w == 7) poll_flags(ctrl + 64, 7, tag, lane);
  bar_vm<0>();
  STAMP(14);
  dma_f(C, 1, 0, 3); dma_f(C, 1, 1, 4); dma_f(C, 1, 2, 5); dma_f(C, 1, 3, 6); dma_f(C, 1, 4, 7); dma_f(C, 1, 5, 8);
  P4_STAGE(oA, 4, 0); P4_STAGE(oA, 5, 1); P4_STAGE(oA, 6, 2);
  bar();
  dma_f(C, 1, 6, 0);
  auto red_write = [&](const v16f& acc, u32 base) {
    const int tk = lane & 31;
#pragma unroll
    for (int a4 = 0; a4 < 4; ++a4)
      *(LAS v4f*)(base + kq * 8192 + tk * 256 + (((8 * fi + 2 * a4 + hf) ^ (tk & 7)) * 16)) = v4f{acc[4 * a4], acc[4 * a4 + 1], acc[4 * a4 + 2], acc[4 * a4 + 3]};
  };
  auto red_final = [&](int a, u32 base) {
    const int tk = tid >> 4, c4 = tid & 15;
    const u32 ra = base + tk * 256 + ((c4 ^ (tk & 7)) * 16);
    const v4f sum = (lds128f(ra) + lds128f(ra + 8192)) + (lds128f(ra + 16384) + lds128f(ra + 24576));
    const v2u hr = lds64(HRES_OFF + (a * 4 + (c4 >> 2)) * 1024 + (((c4 >> 1) & 1) * 32 + tk) * 16 + (c4 & 1) * 8);
    const v2u o = {pk(bflo(hr.x) + sum.x, bfhi(hr.x) + sum.y), pk(bflo(hr.y) + sum.z, bfhi(hr.y) + sum.w)};
    *(GAS v2u*)((GAS char*)out + (size_t)(t0 + 32 * a + tk) * 1024 + c * 128 + c4 * 8) = o;
  };
  red_write(oA, 1 * UNIT);
  bar_vm<8>();
  red_final(0, 1 * UNIT);
  P4_STAGE(oB, 0, 3); P4_STAGE(oB, 1, 4); P4_STAGE(oB, 2, 5);
  bar_vm<1>();
  P4_STAGE(oB, 3, 6); P4_STAGE(oB, 4, 7); P4_STAGE(oB, 5, 8); P4_STAGE(oB, 6, 0);
  red_write(oB, 1 * UNIT);
  bar();
  red_final(1, 1 * UNIT);
  if (c == 0 && tid == 448) __hip_atomic_store(ctrl, tag, RLX_AGENT);
}

extern "C" void kernel_launch(void* const* d_in, const int* in_sizes, int n_in, void* d_out, int out_size, void* d_ws, size_t ws_size,
                              hipStream_t stream) {
  (void)hipFuncSetAttribute((const void*)fused_block, hipFuncAttributeMaxDynamicSharedMemorySize, LDS_BYTES);
  fused_block<<<256, 512, LDS_BYTES, stream>>>(d_in[0], d_in[1], d_in[2], d_in[4], d_in[5], d_out, d_ws);
}
```

```cpp
#pragma clang diagnostic ignored "-Wint-to-pointer-cast"
#include <hip/hip_runtime.h>
#include <hip/hip_bf16.h>
#include <type_traits>

typedef unsigned u32;
typedef unsigned long long u64;
typedef u32 v4u __attribute__((ext_vector_type(4)));
typedef u32 v2u __attribute__((ext_vector_type(2)));
typedef float v4f __attribute__((ext_vector_type(4)));
typedef float v2f __attribute__((ext_vector_type(2)));
typedef float v16f __attribute__((ext_vector_type(16)));
typedef short v4s __attribute__((ext_vector_type(4)));
typedef __bf16 v8bf __attribute__((ext_vector_type(8)));
typedef __bf16 v2bf __attribute__((ext_vector_type(2)));
#define LAS __attribute__((address_space(3)))
#define GAS __attribute__((address_space(1)))
#define DEV __device__ __forceinline__

#define ABL 0
#define PRIO_M 0
#define PRIO_V 2
#define TLBWARM 0
#define KSTAMP 0

#define STAMP(i) do {} while (0)
#define STAMPX(i) do {} while (0)

namespace {
constexpr int SEQ = 2048, DM = 512, FF = 1792;
constexpr u32 O_OFF = 65536, H_OFF = O_OFF + 2097152, F_OFF = H_OFF + 2097152, PS_OFF = F_OFF + 7340032, WS_END = PS_OFF + 524288;
constexpr u32 STAMP_OFF = 16u << 20;
constexpr u32 UNIT = 16384;
constexpr u32 XKC = 10240, WR_OFF = 81920, WSTG = 24576;
constexpr u32 HRES_OFF = 9 * UNIT;
constexpr u32 TOUCH_OFF = 155648;
constexpr u32 CNT_OFF = 155904;
constexpr u32 ST_OFF = 156160;
constexpr u32 CS_OFF = 160256;
constexpr u32 EPO_OFF = 161280;
constexpr u32 LDS_BYTES = 163840;
#define SB() __builtin_amdgcn_sched_barrier(0)
#define PIN(x) asm volatile("" : "+v"(x))

DEV v4u mk_rsrc(const void* p, u32 bytes) {
  u64 a = (u64)p;
  v4u r;
  r.x = (u32)a; r.y = (u32)(a >> 32) & 0xffffu; r.z = bytes; r.w = 0x00020000u;
  return r;
}
template <u32 LI> DEV void dma1(v4u rs, u32 v, u32 so, u32 lb) {
  asm volatile("s_add_u32 m0, %3, %4\n\ts_nop 0\n\tbuffer_load_dwordx4 %0, %1, %2 offen lds" ::"v"(v), "s"(rs), "s"(so), "s"(lb), "n"(LI) : "memory", "scc");
}
template <u32 LI, u32 OF> DEV void dma1o(v4u rs, u32 v, u32 so, u32 lb) {
  asm volatile("s_add_u32 m0, %3, %4\n\ts_nop 0\n\tbuffer_load_dwordx4 %0, %1, %2 offen offset:%5 lds" ::"v"(v), "s"(rs), "s"(so), "s"(lb), "n"(LI), "n"(OF) : "memory", "scc");
}
template <u32 OF> DEV void dma1r(v4u rs, u32 v, u32 so, u32 lds) {
  asm volatile("s_mov_b32 m0, %3\n\ts_nop 0\n\tbuffer_load_dwordx4 %0, %1, %2 offen offset:%4 lds" ::"v"(v), "s"(rs), "s"(so), "s"(lds), "n"(OF) : "memory");
}
template <u32 LI> DEV void dma3(v4u rs, u32 v, u32 so, u32 lb) {
  asm volatile("s_add_u32 m0, %3, %4\n\ts_nop 0\n\tbuffer_load_dwordx4 %0, %1, %2 offen lds\n\t"
               "buffer_load_dwordx4 %0, %1, %2 offen offset:1024 lds\n\t"
               "buffer_load_dwordx4 %0, %1, %2 offen offset:2048 lds" ::"v"(v), "s"(rs), "s"(so), "s"(lb), "n"(LI) : "memory", "scc");
}
template <u32 LI> DEV void dma2v(v4u rs, u32 v0, u32 v1, u32 so, u32 lb) {
  asm volatile("s_add_u32 m0, %4, %5\n\ts_nop 0\n\tbuffer_load_dwordx4 %0, %2, %3 offen lds\n\t"
               "buffer_load_dwordx4 %1, %2, %3 offen offset:1024 lds" ::"v"(v0), "v"(v1), "s"(rs), "s"(so), "s"(lb), "n"(LI) : "memory", "scc");
}
template <u32 LI> DEV void dma4v(v4u rs, const u32 (&v)[4], u32 so, u32 lb) {
  asm volatile("s_add_u32 m0, %6, %7\n\ts_nop 0\n\tbuffer_load_dwordx4 %0, %4, %5 offen lds\n\t"
               "buffer_load_dwordx4 %1, %4, %5 offen offset:1024 lds\n\t"
               "buffer_load_dwordx4 %2, %4, %5 offen offset:2048 lds\n\t"
               "buffer_load_dwordx4 %3, %4, %5 offen offset:3072 lds" ::"v"(v[0]), "v"(v[1]), "v"(v[2]), "v"(v[3]), "s"(rs), "s"(so), "s"(lb), "n"(LI) : "memory", "scc");
}
DEV void dma_halo(v4u rs, u32 v0, u32 v1, u32 so, u32 lds) {
  asm volatile("s_mov_b32 m0, %4\n\ts_mov_b32 exec_lo, 0xffffff00\n\tbuffer_load_dwordx4 %0, %2, %3 offen lds\n\ts_mov_b32 exec_lo, -1\n\t"
               "buffer_load_dwordx4 %1, %2, %3 offen offset:1024 lds" ::"v"(v0), "v"(v1), "s"(rs), "s"(so), "s"(lds) : "memory");
}
DEV void dma4b_sc1(v4u rs, u32 voff, u32 soff, u32 lds) {
  asm volatile("s_mov_b32 m0, %3\n\ts_nop 0\n\tbuffer_load_dword %0, %1, %2 offen sc1 lds" ::"v"(voff), "s"(rs), "s"(soff), "s"(lds) : "memory");
}
template <u32 LI> DEV void lin2_sc1(v4u rs, u32 v, u32 so, u32 lb) {
  asm volatile("s_add_u32 m0, %3, %4\n\ts_nop 0\n\tbuffer_load_dwordx4 %0, %1, %2 offen sc1 lds\n\t"
               "buffer_load_dwordx4 %0, %1, %2 offen offset:1024 sc1 lds" ::"v"(v), "s"(rs), "s"(so), "s"(lb), "n"(LI) : "memory", "scc");
}
template <u32 LI> DEV void lin4_sc1(v4u rs, u32 v, u32 so, u32 lb) {
  asm volatile("s_add_u32 m0, %3, %4\n\ts_nop 0\n\tbuffer_load_dwordx4 %0, %1, %2 offen sc1 lds\n\t"
               "buffer_load_dwordx4 %0, %1, %2 offen offset:1024 sc1 lds\n\t"
               "buffer_load_dwordx4 %0, %1, %2 offen offset:2048 sc1 lds\n\t"
               "buffer_load_dwordx4 %0, %1, %2 offen offset:3072 sc1 lds" ::"v"(v), "s"(rs), "s"(so), "s"(lb), "n"(LI) : "memory", "scc");
}
DEV void touch(v4u rs, u32 voff) {
  asm volatile("s_mov_b32 m0, %2\n\ts_nop 0\n\tbuffer_load_dword %0, %1, 0 offen lds" ::"v"(voff), "s"(rs), "n"(TOUCH_OFF) : "memory");
}
template <int N> DEV void wait_vm() { asm volatile("s_waitcnt vmcnt(%0)" ::"n"(N) : "memory"); }
DEV void bar() { asm volatile("s_waitcnt lgkmcnt(0)\n\ts_barrier" ::: "memory"); }
template <int N> DEV void bar_vm() { asm volatile("s_waitcnt vmcnt(%0) lgkmcnt(0)\n\ts_barrier" ::"n"(N) : "memory"); }

DEV v4u lds128(u32 a) { return *(LAS v4u*)a; }
DEV v2u lds64(u32 a) { return *(LAS v2u*)a; }
DEV v4f lds128f(u32 a) { return *(LAS v4f*)a; }
DEV v2f lds64f(u32 a) { return *(LAS v2f*)a; }
DEV float lds32f(u32 a) { return *(LAS float*)a; }
DEV v2u trd(u32 a) { return __builtin_bit_cast(v2u, __builtin_amdgcn_ds_read_tr16_b64_v4i16((LAS v4s*)a)); }
DEV v4u trd2(u32 a0, u32 a1) { v2u x = trd(a0), y = trd(a1); return v4u{x.x, x.y, y.x, y.y}; }
struct LB {
  u32 b[3];
  DEV explicit LB(u32 lanepart) {
#pragma unroll
    for (int i = 0; i < 3; ++i) { b[i] = lanepart + i * 65536u; asm("" : "+v"(b[i])); }
  }
  DEV u32 operator()(u32 off) const { return b[off >> 16] + (off & 0xffffu); }
};
DEV u32 opq(u32 x) { asm("" : "+v"(x)); return x; }
DEV v4f mfma16(v4u a, v4u b, v4f c) {
  return __builtin_amdgcn_mfma_f32_16x16x32_bf16(__builtin_bit_cast(v8bf, a), __builtin_bit_cast(v8bf, b), c, 0, 0, 0);
}
DEV v4f mfma16k16(v2u a, v2u b, v4f c) {
  return __builtin_amdgcn_mfma_f32_16x16x16bf16_1k(__builtin_bit_cast(v4s, a), __builtin_bit_cast(v4s, b), c, 0, 0, 0);
}
DEV v16f mfma32(v4u a, v4u b, v16f c) {
  return __builtin_amdgcn_mfma_f32_32x32x16_bf16(__builtin_bit_cast(v8bf, a), __builtin_bit_cast(v8bf, b), c, 0, 0, 0);
}
DEV float dot2(u32 a, u32 b, float c) {
  return __builtin_amdgcn_fdot2_f32_bf16(__builtin_bit_cast(v2bf, a), __builtin_bit_cast(v2bf, b), c, false);
}
constexpr u32 ONES = 0x3f803f80u;
DEV void stat1(u32 a, float& s, float& q) {
  float lo, hi;
  asm volatile("v_lshlrev_b32 %2, 16, %4\n\tv_and_b32 %3, 0xffff0000, %4\n\tv_add_f32 %0, %0, %2\n\tv_fmac_f32 %1, %2, %2\n\t"
               "v_add_f32 %0, %0, %3\n\tv_fmac_f32 %1, %3, %3" : "+v"(s), "+v"(q), "=&v"(lo), "=&v"(hi) : "v"(a));
}
DEV void stat2x(u32 a, u32 b, float& sa, float& qa, float& sb, float& qb) {
  float la, ha, lb, hb;
  asm volatile("v_lshlrev_b32 %4, 16, %8\n\tv_lshlrev_b32 %6, 16, %9\n\tv_and_b32 %5, 0xffff0000, %8\n\tv_and_b32 %7, 0xffff0000, %9\n\t"
               "v_add_f32 %0, %0, %4\n\tv_add_f32 %2, %2, %6\n\tv_fmac_f32 %1, %4, %4\n\tv_fmac_f32 %3, %6, %6\n\t"
               "v_add_f32 %0, %0, %5\n\tv_add_f32 %2, %2, %7\n\tv_fmac_f32 %1, %5, %5\n\tv_fmac_f32 %3, %7, %7"
               : "+v"(sa), "+v"(qa), "+v"(sb), "+v"(qb), "=&v"(la), "=&v"(ha), "=&v"(lb), "=&v"(hb) : "v"(a), "v"(b));
}
DEV v4u merge16(v2u a, v2u b) {
  auto x = __builtin_amdgcn_permlane16_swap(a.x, b.x, false, false);
  auto y = __builtin_amdgcn_permlane16_swap(a.y, b.y, false, false);
  return v4u{x[0], y[0], x[1], y[1]};
}
DEV void sum4(v4u f, float& s0, float& s1) {
  u32 a = f.x, b = f.y, c = f.z, d = f.w;
  s0 = dot2(a, ONES, s0); s1 = dot2(b, ONES, s1); s0 = dot2(c, ONES, s0); s1 = dot2(d, ONES, s1); PIN(s0); PIN(s1);
}
DEV u32 pk(float lo, float hi) { v2bf p = {(__bf16)lo, (__bf16)hi}; return __builtin_bit_cast(u32, p); }
DEV float bflo(u32 x) { return __uint_as_float(x << 16); }
DEV float bfhi(u32 x) { return __uint_as_float(x & 0xffff0000u); }
DEV u32 f2u(float x) { return __float_as_uint(x); }
DEV float u2f(u32 x) { return __uint_as_float(x); }
DEV float add16(float x) { auto r = __builtin_amdgcn_permlane16_swap(f2u(x), f2u(x), false, false); return u2f(r[0]) + u2f(r[1]); }
DEV float add32(float x) { auto r = __builtin_amdgcn_permlane32_swap(f2u(x), f2u(x), false, false); return u2f(r[0]) + u2f(r[1]); }
DEV float vadd(float a, float b) { float r; asm("v_add_f32 %0, %1, %2" : "=v"(r) : "v"(a), "v"(b)); return r; }
DEV float vmax(float a, float b) { float r; asm("v_max_f32 %0, %1, %2" : "=v"(r) : "v"(a), "v"(b)); return r; }
DEV float max16(float x) { auto r = __builtin_amdgcn_permlane16_swap(f2u(x), f2u(x), false, false); return vmax(u2f(r[0]), u2f(r[1])); }
DEV float max32(float x) { auto r = __builtin_amdgcn_permlane32_swap(f2u(x), f2u(x), false, false); return vmax(u2f(r[0]), u2f(r[1])); }
template <int CTRL> DEV float dpp_add(float x) { return x + u2f(__builtin_amdgcn_update_dpp(0, f2u(x), CTRL, 0xf, 0xf, true)); }
DEV float red8(float x) { return dpp_add<0x141>(dpp_add<0x4E>(dpp_add<0xB1>(x))); }
DEV float bcast16(float x) { return u2f(__builtin_amdgcn_update_dpp(0, f2u(x), 0x150, 0xf, 0xf, true)); }
DEV float sel(u64 m, float t, float f) { return __builtin_amdgcn_inverse_ballot_w64(m) ? t : f; }
DEV void put_flag(__amdgpu_buffer_rsrc_t rs, u32 off, u64 tag, int lane) {
  __builtin_amdgcn_raw_buffer_store_b64(v2u{(u32)tag, (u32)(tag >> 32)}, rs, lane == 0 ? off : 0x40000000u, 0, 16);
}
DEV void poll_flags(__amdgpu_buffer_rsrc_t rs, u32 off, int n, u64 tag, int lane) {
  const u32 a = lane < n ? off + lane * 8 : 0x40000000u;
  const u64 need = (1ull << n) - 1;
  for (int it = 0; it < 40000; ++it) {
    asm volatile("" ::: "memory");
    const v2u v = __builtin_amdgcn_raw_buffer_load_b64(rs, a, 0, 16);
    if ((__builtin_amdgcn_ballot_w64(v.x == (u32)tag && v.y == (u32)(tag >> 32)) & need) == need) break;
  }
  asm volatile("" ::: "memory");
}
#define TSCHED 1
#define TOUCHES(s) do { if ((s) == 0) touch(rwo, vT0); \
    if ((s) == 1) { if (h == 0) touch(rw1, vT1); } if ((s) == 2) { if (h == 1 || h == 2) touch(rw1, vT1); } \
    if ((s) == 3) { if (h == 3) touch(rw1, vT1); else if (h == 0) touch(rw2, vT2); } if ((s) == 4) { if (h == 1 || h == 2) touch(rw2, vT2); } \
    if ((s) == 5) { if (h == 3) touch(rw2, vT2); } } while (0)
struct Stamps {
};
template <int I, int N, class F> DEV void sfor(F&& f) {
  if constexpr (I < N) { f(std::integral_constant<int, I>{}); sfor<I + 1, N>(f); }
}
}

__global__ void __launch_bounds__(512, 2)
fused_block(const void* __restrict__ x, const void* __restrict__ wqkv, const void* __restrict__ wo, const void* __restrict__ w1,
            const void* __restrict__ w2, void* __restrict__ out, void* __restrict__ ws) {
  extern __shared__ __attribute__((aligned(16))) char smem[];
  Stamps C;
  STAMP(0);
  const int tid = threadIdx.x, lane = tid & 63, w = __builtin_amdgcn_readfirstlane(tid >> 6);
  const int b = blockIdx.x, xcd = b & 7, idx = b >> 3;
  const int sh = idx >> 2, q4 = idx & 3;
  const int tt = 8 * (xcd >> 1) + sh, c = 4 * (xcd & 1) + q4;
  const int rot = (sh + 2 * q4) & 7;
  const v4u rwo = mk_rsrc(wo, DM * DM * 2);
  const v4u rw1 = mk_rsrc(w1, c < 7 ? DM * FF * 2 : 0), rw2 = mk_rsrc(w2, FF * DM * 2), rws = mk_rsrc(ws, WS_END);
  const int t0 = 64 * tt;
  const u32 l3 = lane >> 3, l7 = lane & 7, l4 = lane >> 4;
  const int tok = lane & 15, g = lane >> 4, q = (lane & 15) >> 2, p = lane & 3, hf = lane >> 5, blk = (lane >> 4) & 1;
  const int th = w >> 2, h = w & 3;
  const __amdgpu_buffer_rsrc_t rsW = __builtin_amdgcn_make_buffer_rsrc(ws, 0, WS_END, 0x00020000);
  const __amdgpu_buffer_rsrc_t rsF = __builtin_amdgcn_make_buffer_rsrc(ws, 0, c < 7 ? WS_END : 0, 0x00020000);
  const u32 ctl = (u32)tt * 1024u;
  const u32 lin16 = opq(lane * 16u);
  const u32 ld2k = (u32)w * 2048u;
  u32 vW1[2];
#pragma unroll
  for (int i = 0; i < 2; ++i) vW1[i] = hf * 3584u + (((((lane & 31) >> 2) ^ (2 * i + hf)) * 4 + (lane & 3)) * 16u) + i * 6144u;
  const u32 soW1 = (u32)w * (4 * 3584u) + (u32)c * 512u;
#define DMA_W1(u, unit) dma2v<(unit) * UNIT>(rw1, vW1[0], vW1[1], soW1 + (u) * (32 * 3584u), ld2k)
  const u32 xra = (u32)((c - rot) & 7) * XKC + (16 + 32 * th + tok) * 128 + (((2 * h + (g >> 1)) ^ (tok >> 1)) * 16) + (g & 1) * 8;
  v2u xres[2], epo;

  if (th) {
    __builtin_amdgcn_s_setprio(PRIO_V);
    const v4u rx = mk_rsrc(x, SEQ * DM * 2), rqkv = mk_rsrc(wqkv, DM * 3 * DM * 2);
    const u32 ch0 = (((l7 >> 1) ^ (l4 & 1)) * 2 + (lane & 1)) * 16u, ch1 = (((l7 >> 1) ^ ((l4 & 1) | 2)) * 2 + (lane & 1)) * 16u;
    const u32 vWa = l3 * 3072u + ch0, vWb = l3 * 3072u + ch1 + 8 * 3072u;
    const u32 vXa = l3 * 1024u + ((l7 ^ l4) * 16u), vXb = l3 * 1024u + ((l7 ^ (4 + l4)) * 16u) + 7168u;
    const u32 soW = (u32)h * (16 * 3072u) + (u32)c * 128u, ldW = (u32)h * 6144u;
    const u32 soX = (u32)(t0 + 16 * h) * 1024u, ldX = (u32)(2 + 2 * h) * 1024u;
    const u32 soH = (u32)(tt ? t0 - 16 : 0) * 1024u;
#define KC(s) ((u32)(((s) + rot) & 7))
#define DMA_WA(s) dma3<WR_OFF + ((s) % 3) * WSTG>(rqkv, vWa, soW + KC(s) * (64 * 3072u), ldW)
#define DMA_WB(s) dma3<WR_OFF + ((s) % 3) * WSTG + 3072>(rqkv, vWb, soW + KC(s) * (64 * 3072u), ldW)
#define DMA_X(s) dma2v<(s) * XKC>(rx, vXa, vXb, soX + KC(s) * 128u, ldX)
    DMA_WA(0); DMA_WB(0); DMA_X(0);
    dma_halo(rx, vXa, vXb, soH + KC(h) * 128u, (u32)h * XKC);
    DMA_X(1);
    dma_halo(rx, vXa, vXb, soH + KC(h + 4) * 128u, (u32)(h + 4) * XKC);
    DMA_WA(1); DMA_WB(1);
    if (w == 7) dma4b_sc1(rws, (lane & 1) * 4u, (u32)tt * 1024u, EPO_OFF);
    const u32 vWo0 = l3 * 1024u + ch0, vWo1 = l3 * 1024u + ch1 + 7168u;
    const u32 soWo = (u32)h * 16384u + (u32)c * 128u, ldo = (u32)h * 2048u;
#define DMA_WO(pp) dma2v<(pp) * 8192>(rwo, vWo0, vWo1, soWo + (pp) * 65536u, ldo)
#define DMA_W1H(u, unit) do { dma2v<(unit) * UNIT>(rw1, vW1[0], vW1[1], soW1 + (u) * (32 * 3584u), ld2k); \
                              dma2v<(unit) * UNIT - 8192>(rw1, vW1[0], vW1[1], soW1 - 16 * 3584u + (u) * (32 * 3584u), ld2k); } while (0)
    const u32 Lw = (u32)(h * 64 + lane) * 8 + sh, Lz = (u32)(h * 56 + (lane < 56 ? lane : 55)) * 8 + sh, L0 = (u32)(h * 16 + (lane >> 2)) * 8 + sh;
    const u32 vT1 = (Lw >> 2) * 3584u + (u32)c * 512u + (Lw & 3) * 128u, vT2 = Lz * 1024u + (u32)c * 128u, vT0 = L0 * 1024u + (u32)c * 128u + (lane & 3) * 32u;
    const u32 xl = (16 + 16 * h + tok) * 128, x0l = tok ? xl : 0;
    const LB xb0(xl + ((g ^ (tok >> 1)) * 16)), xb1(xl + (((4 + g) ^ (tok >> 1)) * 16));
    const LB xz0(x0l + ((g ^ (tok >> 1)) * 16)), xz1(x0l + (((4 + g) ^ (tok >> 1)) * 16));
    const u32 shl = opq((l3 & 1) * XKC + (4 * h + g) * 128 + l7 * 16);
    float sa = 0, qa = 0, sb = 0, qb = 0;
    v4f G = {0, 0, 0, 0};
    auto fin = [&](float s_, float q_, u32 a) {
      const float mean = s_ * (1.f / DM);
      const float rs = __builtin_amdgcn_rsqf(vmax(q_ * (1.f / DM) - mean * mean, 0.f) + 1e-5f);
      *(LAS float*)a = mean; *(LAS float*)(a + 320) = rs;
    };
#define GRAM(s) do { G = mfma16(lds128(xz0((s) * XKC)), lds128(xb0((s) * XKC)), G); G = mfma16(lds128(xz1((s) * XKC)), lds128(xb1((s) * XKC)), G); PIN(G); } while (0)
    v4u hx[4];
#define S2(i, d) do { const u32 a_ = hx[i][d], b_ = hx[(i) + 1][d]; stat2x(a_, b_, sa, qa, sb, qb); } while (0)
    STAMP(1);
    bar_vm<6>();
    STAMP(17);
    DMA_WA(2); DMA_WB(2);
#pragma unroll
    for (int i = 0; i < 4; ++i) hx[i] = lds128(shl + 2 * i * XKC);
    STAMP(18);
    bar_vm<6>();
    DMA_X(2); DMA_WA(3); S2(0, 0); DMA_WB(3); S2(0, 1); DMA_X(3); TOUCHES(0); TOUCHES(1);
    epo = lds64(EPO_OFF);
    STAMP(19);
    bar_vm<8>();
    DMA_X(4); GRAM(0); DMA_WA(4); S2(0, 2); DMA_WB(4); S2(0, 3); DMA_X(5); TOUCHES(2);
    STAMP(20);
    bar_vm<10>();
    DMA_X(6); GRAM(1); DMA_WA(5); S2(2, 0); DMA_WB(5); S2(2, 1); DMA_X(7); GRAM(2); TOUCHES(3);
    STAMP(21);
    bar_vm<10>();
    STAMP(25);
    DMA_WA(6); GRAM(3); S2(2, 2); DMA_WB(6); GRAM(4); S2(2, 3); GRAM(5); TOUCHES(4);
    STAMP(22);
    bar_vm<6>();
    DMA_WA(7);
    const v4u r0 = lds128(l3 * XKC + (16 + 16 * h) * 128 + l7 * 16);
    xres[0] = lds64(xra); xres[1] = lds64(xra + 2048);
    GRAM(6); DMA_WB(7); GRAM(7); TOUCHES(5);
    fin(dpp_add<0x140>(red8(sa + sb)), dpp_add<0x140>(red8(qa + qb)), ST_OFF + (4 * h + g) * 4);
    STAMP(23);
    bar_vm<6>();
    DMA_WO(0); DMA_WO(1);
    {
      sa = 0; qa = 0; sb = 0; qb = 0;
      const u32 a = r0.x, b2 = r0.y, c2 = r0.z, d = r0.w;
      stat2x(a, b2, sa, qa, sb, qb); stat2x(c2, d, sa, qa, sb, qb);
      DMA_WO(2); DMA_WO(3);
      const float q0 = add32(add16(dpp_add<0x140>(red8(qa + qb))));
      auto r = __builtin_amdgcn_permlane16_swap(f2u(G.x), f2u(G.x), false, false);
      auto r2 = __builtin_amdgcn_permlane32_swap(r[0], r[0], false, false);
      const float dg = (tok & 2) ? ((tok & 1) ? G.w : G.z) : ((tok & 1) ? G.y : G.x);
      fin(u2f(r2[0]), tok ? dg : q0, g == (tok >> 2) ? ST_OFF + (16 + 16 * h + tok) * 4 : ST_OFF + 1024);
    }
    DMA_WO(4); DMA_WO(5);
    STAMP(24);
    bar_vm<12>();
    DMA_WO(6); DMA_WO(7); DMA_W1H(0, 5); DMA_W1H(1, 8);
    STAMP(2);
  } else {
    __builtin_amdgcn_s_setprio(PRIO_M);
    if (w == 0) *(LAS v4u*)(l3 * XKC + l7 * 16) = v4u{ONES, ONES, ONES, ONES};
    v4f aK[5], aV[5], aQ[5];
#pragma unroll
    for (int i = 0; i < 5; ++i) { aK[i] = v4f{0, 0, 0, 0}; aV[i] = v4f{0, 0, 0, 0}; aQ[i] = v4f{0, 0, 0, 0}; }
    const LB xa0(tok * 128 + ((g ^ (tok >> 1)) * 16)), xa1(tok * 128 + (((4 + g) ^ (tok >> 1)) * 16));
    const LB wa(g * 3072 + q * 128 + ((h ^ ((q >> 1) | ((g & 1) << 1))) * 32) + 8 * p);
    const int d0 = tok - 4 * g - (tok & 1);
    const bool odd = tok & 1, cur0 = d0 >= 0, cur1 = d0 >= 2;
    const u64 mOdd = __builtin_amdgcn_ballot_w64(odd), mC0 = __builtin_amdgcn_ballot_w64(cur0), mC1 = __builtin_amdgcn_ballot_w64(cur1);
    u32 selP[4];
    {
      const u32 s0 = odd ? 0x01000c0cu : 0x0c0c0100u, s1 = odd ? 0x03020c0cu : 0x0c0c0302u, z = 0x0c0c0c0cu;
      selP[0] = cur0 ? z : s0; selP[1] = cur1 ? z : s1; selP[2] = cur0 ? s0 : z; selP[3] = cur1 ? s1 : z;
      PIN(selP[0]); PIN(selP[1]); PIN(selP[2]); PIN(selP[3]);
    }
    u32 stT = ST_OFF + tok * 4, stG = ST_OFF + 4 * g * 4;
    u32 stK0 = ST_OFF + 320 + (4 * g + (tok & 1) + (cur0 ? 16 : 0)) * 4;
    u32 stK1 = ST_OFF + 320 + (4 * g + (tok & 1) + 2 + (cur1 ? 16 : 0)) * 4;
    const bool first = tt == 0;
    float keep0 = (first && !cur0) ? 0.f : 1.f, keep1 = (first && !cur1) ? 0.f : 1.f;
    float bias0 = (first && !cur0) ? -1.44269504e9f : 0.f, bias1 = (first && !cur1) ? -1.44269504e9f : 0.f;
    u32 ooff = O_OFF + (u32)(((tt * 8 + c) * 4 + (g & 1)) * 2 + (h >> 1)) * 1024u + (u32)((2 * (h & 1) + (g >> 1)) * 16 + tok) * 16u;
    PIN(stT); PIN(stG); PIN(stK0); PIN(stK1); PIN(keep0); PIN(keep1); PIN(bias0); PIN(bias1); PIN(ooff);
    float mt[5], rq[4], rho[4][2], fac[4][2];
    v4f m4[5];
    v4u fx[2][5], fq[2], fk[2], fv[2];
    STAMP(1);
    bar();
    STAMP(17);
    fk[0] = trd2(wa(WR_OFF + 1024), wa(WR_OFF + 1024 + 512));
#pragma unroll
    for (int i = 0; i < 5; ++i) fx[0][i] = lds128(xa0(i * 2048));
    fq[0] = trd2(wa(WR_OFF), wa(WR_OFF + 512));
    fv[0] = trd2(wa(WR_OFF + 2048), wa(WR_OFF + 2048 + 512));
    sfor<0, 16>([&](auto kk) {
      constexpr int K = decltype(kk)::value, N = K + 1, sN = N >> 1, ksN = N & 1, A = K & 1, B = N & 1;
      if constexpr ((K & 1) && K < 15) { STAMPX(17 + sN); bar(); }
      if constexpr (K == 7) STAMP(25);
      constexpr u32 xb = sN * XKC, wb = WR_OFF + (sN % 3) * WSTG + ksN * 12288;
      const LB& xa = ksN ? xa1 : xa0;
      constexpr bool R = K < 15;
      SB();
      aK[0] = mfma16(fk[A], fx[A][0], aK[0]); if constexpr (R) fk[B] = trd2(wa(wb + 1024), wa(wb + 1024 + 512));
      if constexpr (K == 15) { m4[0] = lds128f(stG); m4[1] = lds128f(stG + 64); }
      SB();
      aK[1] = mfma16(fk[A], fx[A][1], aK[1]); if constexpr (R) fx[B][0] = lds128(xa(xb));
      if constexpr (K == 15) { m4[2] = lds128f(stG + 128); m4[3] = lds128f(stG + 192); }
      SB();
      aK[2] = mfma16(fk[A], fx[A][2], aK[2]); if constexpr (R) fx[B][1] = lds128(xa(xb + 2048));
      if constexpr (K == 15) m4[4] = lds128f(stG + 256);
      SB();
      aK[3] = mfma16(fk[A], fx[A][3], aK[3]); if constexpr (R) fx[B][2] = lds128(xa(xb + 4096));
      if constexpr (K == 15) { for (int i = 0; i < 5; ++i) mt[i] = lds32f(stT + 64 * i); }
      SB();
      aK[4] = mfma16(fk[A], fx[A][4], aK[4]); if constexpr (R) fx[B][3] = lds128(xa(xb + 6144));
      SB();
      aQ[0] = mfma16(fq[A], fx[A][0], aQ[0]); if constexpr (R) fx[B][4] = lds128(xa(xb + 8192));
      SB();
      aQ[1] = mfma16(fq[A], fx[A][1], aQ[1]); if constexpr (R) fq[B] = trd2(wa(wb), wa(wb + 512));
      SB();
      aQ[2] = mfma16(fq[A], fx[A][2], aQ[2]); if constexpr (R) fv[B] = trd2(wa(wb + 2048), wa(wb + 2048 + 512)); SB();
      aQ[3] = mfma16(fq[A], fx[A][3], aQ[3]);
      if constexpr (K == 9) { xres[0] = lds64(xra); xres[1] = lds64(xra + 2048); epo = lds64(EPO_OFF); }
      if constexpr (K == 13) { for (int i = 0; i < 4; ++i) { rq[i] = lds32f(stT + 320 + 64 * (i + 1)); } }
      if constexpr (K == 14) { for (int i = 0; i < 4; ++i) { rho[i][0] = lds32f(stK0 + 64 * i); rho[i][1] = lds32f(stK1 + 64 * i); } }
      SB();
      aQ[4] = mfma16(fq[A], fx[A][4], aQ[4]);
      if constexpr (K == 15) { rq[0] *= 0.25f * 1.44269504f; PIN(rq[0]); rq[1] *= 0.25f * 1.44269504f; PIN(rq[1]); }
      SB();
      aV[0] = mfma16(fx[A][0], fv[A], aV[0]);
      if constexpr (K == 15) { rq[2] *= 0.25f * 1.44269504f; PIN(rq[2]); rq[3] *= 0.25f * 1.44269504f; PIN(rq[3]); }
      SB();
      aV[1] = mfma16(fx[A][1], fv[A], aV[1]);
      if constexpr (K == 15) { fac[0][0] = rho[0][0] * rq[0] * keep0; PIN(fac[0][0]); fac[0][1] = rho[0][1] * rq[0] * keep1; PIN(fac[0][1]); }
      SB();
      aV[2] = mfma16(fx[A][2], fv[A], aV[2]);
      if constexpr (K == 15) { fac[1][0] = rho[1][0] * rq[1]; PIN(fac[1][0]); fac[1][1] = rho[1][1] * rq[1]; PIN(fac[1][1]); }
      SB();
      aV[3] = mfma16(fx[A][3], fv[A], aV[3]);
      if constexpr (K == 15) { fac[2][0] = rho[2][0] * rq[2]; PIN(fac[2][0]); fac[2][1] = rho[2][1] * rq[2]; PIN(fac[2][1]); }
      SB();
      aV[4] = mfma16(fx[A][4], fv[A], aV[4]);
      if constexpr (K == 15) { fac[3][0] = rho[3][0] * rq[3]; PIN(fac[3][0]); fac[3][1] = rho[3][1] * rq[3]; PIN(fac[3][1]); }
      SB();
    });
    STAMP(2);
    SB();
    const v4f ck = {bcast16(aK[0].x), bcast16(aK[0].y), bcast16(aK[0].z), bcast16(aK[0].w)};
    const v4f cq = {bcast16(aQ[0].x), bcast16(aQ[0].y), bcast16(aQ[0].z), bcast16(aQ[0].w)};
    v2u Kp[5], Vp[5], Qp[4];
#pragma unroll
    for (int i = 0; i < 5; ++i) {
      const v4f k = aK[i] - ck * mt[i];
      Kp[i] = v2u{pk(k.x, k.y), pk(k.z, k.w)};
      if (i > 0) { const v4f qv = aQ[i] - cq * mt[i]; Qp[i - 1] = v2u{pk(qv.x, qv.y), pk(qv.z, qv.w)}; }
    }
    const v4f z = {0, 0, 0, 0};
    v4f sp[4], sc[4];
#pragma unroll
    for (int i = 0; i < 4; ++i) { sp[i] = mfma16k16(Kp[i], Qp[i], z); sc[i] = mfma16k16(Kp[i + 1], Qp[i], z); }
    float cv;
    {
      auto r = __builtin_amdgcn_permlane16_swap(f2u(aV[0].x), f2u(aV[0].x), false, false);
      auto r2 = __builtin_amdgcn_permlane32_swap(r[0], r[0], false, false);
      cv = u2f(r2[0]);
    }
#pragma unroll
    for (int i = 0; i < 5; ++i) { const v4f v = aV[i] - m4[i] * cv; Vp[i] = v2u{pk(v.x, v.y), pk(v.z, v.w)}; }
    float e0[4], e1[4], mx[4];
#pragma unroll
    for (int i = 0; i < 4; ++i) {
      e0[i] = sel(mC0, sel(mOdd, sc[i].y, sc[i].x), sel(mOdd, sp[i].y, sp[i].x)) * fac[i][0];
      e1[i] = sel(mC1, sel(mOdd, sc[i].w, sc[i].z), sel(mOdd, sp[i].w, sp[i].z)) * fac[i][1];
      if (i == 0) { e0[0] += bias0; e1[0] += bias1; }
      mx[i] = vmax(e0[i], e1[i]);
    }
#define RED4(v, OP, POST) do { \
      auto r01 = __builtin_amdgcn_permlane16_swap(f2u(v[0]), f2u(v[1]), false, false); \
      auto r23 = __builtin_amdgcn_permlane16_swap(f2u(v[2]), f2u(v[3]), false, false); \
      auto rr = __builtin_amdgcn_permlane32_swap(f2u(OP(u2f(r01[0]), u2f(r01[1]))), f2u(OP(u2f(r23[0]), u2f(r23[1]))), false, false); \
      const float t_ = POST(OP(u2f(rr[0]), u2f(rr[1])));     \
      auto g_ = __builtin_amdgcn_permlane32_swap(f2u(t_), f2u(t_), false, false); \
      auto ga = __builtin_amdgcn_permlane16_swap(g_[0], g_[0], false, false); \
      auto gb = __builtin_amdgcn_permlane16_swap(g_[1], g_[1], false, false); \
      v[0] = u2f(ga[0]); v[1] = u2f(ga[1]); v[2] = u2f(gb[0]); v[3] = u2f(gb[1]); } while (0)
#define OP_ADD(a, b) ((a) + (b))
#define OP_ID(a) (a)
    RED4(mx, vmax, OP_ID);
    v4f o4[4];
    float sm[4];
#pragma unroll
    for (int i = 0; i < 4; ++i) {
      e0[i] = __builtin_amdgcn_exp2f(e0[i] - mx[i]); e1[i] = __builtin_amdgcn_exp2f(e1[i] - mx[i]);
      const u32 pp = pk(e0[i] * rho[i][0], e1[i] * rho[i][1]);
      const v4u P = {__builtin_amdgcn_perm(0u, pp, selP[0]), __builtin_amdgcn_perm(0u, pp, selP[1]), __builtin_amdgcn_perm(0u, pp, selP[2]), __builtin_amdgcn_perm(0u, pp, selP[3])};
      o4[i] = mfma16(v4u{Vp[i].x, Vp[i].y, Vp[i + 1].x, Vp[i + 1].y}, P, z);
      sm[i] = e0[i] + e1[i];
    }
    RED4(sm, OP_ADD, __builtin_amdgcn_rcpf);
    v2u op[4];
#pragma unroll
    for (int i = 0; i < 4; ++i) { const v4f o = o4[i] * sm[i]; op[i] = v2u{pk(o.x, o.y), pk(o.z, o.w)}; }
    __builtin_amdgcn_raw_buffer_store_b128(merge16(op[0], op[1]), rsW, ooff, 0, 16);
    __builtin_amdgcn_raw_buffer_store_b128(merge16(op[2], op[3]), rsW, ooff + 4096, 0, 16);
    __builtin_amdgcn_s_setprio(PRIO_V);
  }
  u64 tag;
  {
    const u64 ep = ((u64)(u32)__builtin_amdgcn_readfirstlane(epo.y) << 32) | (u32)__builtin_amdgcn_readfirstlane(epo.x);
    tag = ep * 0x9E3779B97F4A7C15ull + 0x632BE59BD9B4E019ull;
  }
  STAMP(3);
  bar();
  DMA_W1(2, 4); DMA_W1(3, 6);
  bar_vm<0>();
  if (w == 7) put_flag(rsW, ctl + 128 + c * 8, tag, lane);
  STAMP(4);
  v4u wof[16], w1f[32];
  v4f csa = {0, 0, 0, 0};
  const u32 selv = ((tok ^ g) & 1) ? 0u : ONES;
  const v4u selB = {selv, selv, selv, selv};
  const LB w1a((8 * hf + q) * 512 + ((w ^ q) * 64) + 32 * blk + 8 * p);
  const LB lin(lane * 16);
#define RD_W1(u, unit) do { w1f[2 * (u)] = trd2(w1a((unit) * UNIT), w1a((unit) * UNIT + 2048)); \
                            w1f[2 * (u) + 1] = trd2(w1a((unit) * UNIT + 8192), w1a((unit) * UNIT + 8192 + 2048)); } while (0)
#define CS_W1(u) do { csa = mfma16(w1f[2 * (u)], selB, csa); csa = mfma16(w1f[2 * (u) + 1], selB, csa); PIN(csa); } while (0)
  RD_W1(0, 5); RD_W1(1, 8);
  DMA_W1(4, 7);
  bar();
  DMA_W1(5, 5); DMA_W1(6, 8);
  {
    const u32 wa = (8 * g + q) * 128 + ((h ^ ((q >> 1) | ((g & 1) << 1))) * 32) + 8 * p;
#pragma unroll
    for (int ks = 0; ks < 16; ++ks) wof[ks] = trd2(wa + ks * 4096, wa + ks * 4096 + 512);
  }
  RD_W1(2, 4); RD_W1(3, 6);
  CS_W1(0); CS_W1(1); CS_W1(2); CS_W1(3);
  if (w == 7) poll_flags(rsW, ctl + 128, 8, tag, lane);
  bar_vm<0>();
  STAMP(5);
  {
    const u32 so = O_OFF + (u32)(tt * 8 + w) * 8192u;
    lin4_sc1<0>(rws, lin16, so, (u32)w * 8192u);
    lin4_sc1<4096>(rws, lin16, so + 4096u, (u32)w * 8192u);
  }
  RD_W1(4, 7); RD_W1(5, 5); RD_W1(6, 8);
  CS_W1(4); CS_W1(5); CS_W1(6);
  DMA_W1(7, 4);
  u32 hoff = lin16 + H_OFF + (u32)((tt * 2 + th) * 32 + 4 * c + h) * 1024u;
  u32 poff = PS_OFF + (u32)(tt * 32 + 4 * c + h) * 512u + th * 256u + lane * 8u + (lane >= 32 ? 0x40000000u : 0u);
  v4f xr0 = {bflo(xres[0].x), bfhi(xres[0].x), bflo(xres[0].y), bfhi(xres[0].y)}, xr1 = {bflo(xres[1].x), bfhi(xres[1].x), bflo(xres[1].y), bfhi(xres[1].y)};
  PIN(hoff); PIN(poff); PIN(xr0); PIN(xr1);
  bar_vm<2>();
  STAMP(6);
  {
    v4f a0 = xr0, a1 = xr1;
    const u32 oa = opq(th * 4096 + lane * 16);
    v4u r[8];
#define O_AD(i) (((i) >> 2) * 8192 + (((i) >> 1) & 1) * 1024 + ((i) & 1) * 2048)
#pragma unroll
    for (int i = 0; i < 8; ++i) r[i] = lds128(oa + O_AD(i));
    SB();
    sfor<0, 32>([&](auto ii) {
      constexpr int I = decltype(ii)::value;
      if constexpr (I & 1) a1 = mfma16(wof[I >> 1], r[I & 7], a1); else a0 = mfma16(wof[I >> 1], r[I & 7], a0);
      if constexpr (I + 8 < 32) r[I & 7] = lds128(oa + O_AD(I + 8));
      if constexpr (I == 1) DMA_W1(8, 6);
      if constexpr (I == 8) DMA_W1(9, 7);
      if constexpr (I == 15) DMA_W1(10, 5);
      SB();
    });
    const v4u h16 = merge16(v2u{pk(a0.x, a0.y), pk(a0.z, a0.w)}, v2u{pk(a1.x, a1.y), pk(a1.z, a1.w)});
    __builtin_amdgcn_raw_buffer_store_b128(h16, rsW, hoff, 0, 16);
    const float s0 = (a0.x + a0.y) + (a0.z + a0.w), s1 = (a1.x + a1.y) + (a1.z + a1.w);
    const float q0 = (a0.x * a0.x + a0.y * a0.y) + (a0.z * a0.z + a0.w * a0.w), q1 = (a1.x * a1.x + a1.y * a1.y) + (a1.z * a1.z + a1.w * a1.w);
    auto rs_ = __builtin_amdgcn_permlane16_swap(f2u(s0), f2u(s1), false, false);
    auto rq_ = __builtin_amdgcn_permlane16_swap(f2u(q0), f2u(q1), false, false);
    const v2u ps = {f2u(add32(u2f(rs_[0]) + u2f(rs_[1]))), f2u(add32(u2f(rq_[0]) + u2f(rq_[1])))};
    __builtin_amdgcn_raw_buffer_store_b64(ps, rsW, poff, 0, 16);
    *(LAS v4u*)(HRES_OFF + w * 1024 + lane * 16) = h16;
  }
  STAMP(7);
  bar();
  DMA_W1(11, 8); DMA_W1(12, 0);
  bar_vm<0>();
  if (w == 7) put_flag(rsW, ctl + 256 + c * 8, tag, lane);
  STAMP(8);
  RD_W1(7, 4);
  DMA_W1(13, 1); DMA_W1(14, 2); DMA_W1(15, 3);
  RD_W1(8, 6); RD_W1(9, 7); RD_W1(10, 5); RD_W1(11, 8); RD_W1(12, 0);
  CS_W1(7); CS_W1(8); CS_W1(9); CS_W1(10); CS_W1(11); CS_W1(12);
  u32 vW2[4];
#pragma unroll
  for (int i = 0; i < 4; ++i) vW2[i] = opq(l3 * 1024u + ((l7 ^ ((l4 & 1) << 2)) * 16u) + i * 7168u);
  const int kq = w >> 1, fi = w & 1;
  const u32 soW2 = (u32)(448 * (h >> 1) + 32 * (h & 1)) * 1024u + (u32)c * 128u, ld4k = (u32)h * 4096u;
#define DMA_W2(G, v, unit) dma4v<(unit) * UNIT>(rw2, vW2, soW2 + ((G) * 896 + (v) * 64) * 1024u, ld4k)
  if (w == 7) poll_flags(rsW, ctl + 256, 8, tag, lane);
  bar_vm<0>();
  STAMP(9);
  constexpr u32 HT = 5 * UNIT;
  {
    const u32 so = H_OFF + (u32)tt * 65536u + (u32)w * 4096u;
    lin4_sc1<HT>(rws, lin16, so, (u32)w * 4096u);
    if (th) lin4_sc1<4 * UNIT>(rws, lin16, PS_OFF + (u32)tt * 16384u + ld4k, ld4k);
    lin4_sc1<HT + 32768>(rws, lin16, so + 32768u, (u32)w * 4096u);
  }
  RD_W1(13, 1); RD_W1(14, 2); RD_W1(15, 3);
  CS_W1(13); CS_W1(14); CS_W1(15);
  v4f cs4[4];
  *(LAS v4f*)(CS_OFF + w * 128 + (tok & 1) * 64 + g * 16) = csa;
#pragma unroll
  for (int j = 0; j < 4; ++j) cs4[j] = lds128f(CS_OFF + w * 128 + j * 32 + hf * 16);
  if (th) wait_vm<8>(); else wait_vm<4>();
  bar();
  STAMP(10);
  v16f acc;
  float mnB, k1B, irB;
  u32 fo[8];
  v4u w2f[28];
  const u32 w2l = (kq & 1) * 8192 + (4 * hf + q) * 128 + ((fi ^ (q >> 1)) * 64) + 32 * blk + 8 * p;
  const u32 w2a = opq(w2l + th * 3 * UNIT), w2b = opq(w2l + (th ? 0 : 6 * UNIT));
#define RD_W2F(v, i, base) w2f[4 * (v) + (i)] = trd2((base) + (i) * 2048, (base) + (i) * 2048 + 1024)
#define RD_W2(v, base) do { RD_W2F(v, 0, base); RD_W2F(v, 1, base); RD_W2F(v, 2, base); RD_W2F(v, 3, base); } while (0)
#define EPI4(mean, k1, irs, j) do { float u_[4], e_[4]; \
    _Pragma("unroll") for (int r = 0; r < 4; ++r) { u_[r] = acc[4 * (j) + r] - cs4[j][r] * mean; PIN(u_[r]); } \
    _Pragma("unroll") for (int r = 0; r < 4; ++r) { e_[r] = u_[r] * k1; PIN(e_[r]); } \
    _Pragma("unroll") for (int r = 0; r < 4; ++r) { e_[r] = __builtin_amdgcn_exp2f(e_[r]); PIN(e_[r]); } \
    _Pragma("unroll") for (int r = 0; r < 4; ++r) { e_[r] = e_[r] * irs + irs; PIN(e_[r]); } \
    _Pragma("unroll") for (int r = 0; r < 4; ++r) { e_[r] = __builtin_amdgcn_rcpf(e_[r]); PIN(e_[r]); } \
    _Pragma("unroll") for (int r = 0; r < 4; ++r) { u_[r] *= e_[r]; PIN(u_[r]); } \
    fo[2 * (j)] = pk(u_[0], u_[1]); fo[2 * (j) + 1] = pk(u_[2], u_[3]); } while (0)
  const u32 foff = opq(lin16 + F_OFF + (u32)(tt * 2 * 112 + 16 * c + 2 * w) * 1024u);
  v4u r_[8];
#define P3_PRE(a) do { _Pragma("unroll") for (int i = 0; i < 8; ++i) r_[i] = lds128(lin(HT + (a) * 32768 + i * 1024)); } while (0)
  P3_PRE(0);
  if (th) {
    wait_vm<4>();
    v2f t[8];
#pragma unroll
    for (int j = 0; j < 8; ++j) t[j] = lds64f(4 * UNIT + ld4k + j * 512 + lane * 8);
    const float s_ = vadd(vadd(vadd(t[0].x, t[1].x), vadd(t[2].x, t[3].x)), vadd(vadd(t[4].x, t[5].x), vadd(t[6].x, t[7].x)));
    const float q_ = vadd(vadd(vadd(t[0].y, t[1].y), vadd(t[2].y, t[3].y)), vadd(vadd(t[4].y, t[5].y), vadd(t[6].y, t[7].y)));
    *(LAS v2f*)(ST_OFF + h * 512 + lane * 8) = v2f{s_, q_};
    STAMP(46);
    bar_vm<0>();
  }
#define W2SRC(G, v) (soW2 + ((G) * 896 + (v) * 64) * 1024u)
#define W2P(i, so, unit) dma1r<(i) * 1024>(rw2, vW2[i], so, (unit) * UNIT + ld4k)
#define P3_LOOP(a, EXTRA) do { \
    __builtin_amdgcn_s_setprio(PRIO_M); \
    SB(); \
    sfor<0, 32>([&](auto ii) { constexpr int I = decltype(ii)::value; \
      if constexpr (I == 0) { v16f z_; _Pragma("unroll") for (int i = 0; i < 16; ++i) z_[i] = 0.f; acc = mfma32(w1f[0], r_[0], z_); } \
      else acc = mfma32(w1f[I], r_[I & 7], acc); \
      PIN(acc); \
      if constexpr (I + 8 < 32) r_[I & 7] = lds128(lin(HT + (a) * 32768 + (I + 8) * 1024)); \
      EXTRA \
      SB(); }); \
    __builtin_amdgcn_s_setprio(PRIO_V); } while (0)
  STAMP(42);
  {
    const u32 sB = th ? W2SRC(1, 0) : W2SRC(0, 0), sC = sB + 65536u, uB = th ? 3 : 0;
    if (h & 1) __builtin_amdgcn_s_sleep(0);
    P3_LOOP(0,
      if constexpr (I < 10 && (I * 2) % 5 < 2) { if (th) W2P((I * 2) / 5, W2SRC(0, 2), 2); }
      else if constexpr (I >= 10 && I < 20 && (I * 2) % 5 < 2) W2P(((I - 10) * 2) / 5, sB, uB);
      else if constexpr (I >= 20 && I < 30 && (I * 2) % 5 < 2) W2P(((I - 20) * 2) / 5, sC, uB + 1);
    );
  }
  STAMP(43);
  bar_vm<8>();
  STAMP(40);
  {
    v2f t[4];
#pragma unroll
    for (int j = 0; j < 4; ++j) t[j] = lds64f(ST_OFF + j * 512 + lane * 8);
    const float s_ = vadd(vadd(t[0].x, t[1].x), vadd(t[2].x, t[3].x));
    const float q_ = vadd(vadd(t[0].y, t[1].y), vadd(t[2].y, t[3].y));
    float mean = s_ * (1.f / DM); PIN(mean);
    const float ve = vmax(q_ * (1.f / DM) - mean * mean, 0.f) + 1e-5f;
    const float rs = __builtin_amdgcn_rsqf(ve);
    auto a_ = __builtin_amdgcn_permlane32_swap(f2u(mean), f2u(mean), false, false);
    auto b_ = __builtin_amdgcn_permlane32_swap(f2u(rs * -1.44269504f), f2u(rs * -1.44269504f), false, false);
    auto c_ = __builtin_amdgcn_permlane32_swap(f2u(ve * rs), f2u(ve * rs), false, false);
    const float mnA = u2f(a_[0]), k1A = u2f(b_[0]), irA = u2f(c_[0]);
    mnB = u2f(a_[1]); k1B = u2f(b_[1]); irB = u2f(c_[1]);
    PIN(mnB); PIN(k1B); PIN(irB); STAMP(26);
    EPI4(mnA, k1A, irA, 0); EPI4(mnA, k1A, irA, 1); EPI4(mnA, k1A, irA, 2); EPI4(mnA, k1A, irA, 3);
    STAMP(38);
    __builtin_amdgcn_raw_buffer_store_b128(v4u{fo[0], fo[1], fo[2], fo[3]}, rsF, foff, 0, 16);
    __builtin_amdgcn_raw_buffer_store_b128(v4u{fo[4], fo[5], fo[6], fo[7]}, rsF, foff + 1024, 0, 16);
    P3_PRE(1);
  }
  STAMP(44);
  bar_vm<2>();
  STAMP(41);
  {
    const u32 sA = th ? W2SRC(1, 3) : W2SRC(1, 2), sB = th ? W2SRC(0, 4) : W2SRC(0, 3), uA = th ? 0 : 5;
    P3_LOOP(1,
      if constexpr (I >= 4 && I < 28 && !(I & 1)) RD_W2F((I - 4) >> 3, ((I - 4) >> 1) & 3, w2a + ((I - 4) >> 3) * UNIT);
      if constexpr (I < 8 && (I & 1)) W2P(I >> 1, sA, uA);
      else if constexpr (I >= 8 && I < 16 && (I & 1)) W2P((I - 8) >> 1, sB, uA + 1);
      else if constexpr (I >= 16 && I < 24 && (I & 1)) { if (th) W2P((I - 16) >> 1, W2SRC(0, 5), 2); }
    );
  }
  STAMP(45);
  bar_vm<0>();
  STAMP(11);
  if (w == 7) put_flag(rsW, ctl + 384 + c * 8, tag, lane);
  EPI4(mnB, k1B, irB, 0); EPI4(mnB, k1B, irB, 1); EPI4(mnB, k1B, irB, 2); EPI4(mnB, k1B, irB, 3);
  STAMP(39);
  __builtin_amdgcn_raw_buffer_store_b128(v4u{fo[0], fo[1], fo[2], fo[3]}, rsF, foff + 112 * 1024u, 0, 16);
  __builtin_amdgcn_raw_buffer_store_b128(v4u{fo[4], fo[5], fo[6], fo[7]}, rsF, foff + 113 * 1024u, 0, 16);
  RD_W2(3, w2b);
  STAMP(32);
  if (!th) {
    bar();
    __builtin_amdgcn_s_sleep(1);
    DMA_W2(0, 6, 3); DMA_W2(1, 4, 4);
  }
  bar();
  if (!th) { DMA_W2(1, 5, 5); DMA_W2(1, 6, 6); }
  bar_vm<0>();
  if (w == 3) put_flag(rsW, ctl + 512 + c * 8, tag, lane);
  STAMP(12);
  RD_W2(4, w2a + UNIT); RD_W2(5, w2a + 2 * UNIT); RD_W2(6, w2a + 3 * UNIT);
  if (w == 3) poll_flags(rsW, ctl + 384, 7, tag, lane);
  bar();
  STAMP(13);
  const u32 soF = F_OFF + (u32)(tt * 2 * 112 + 28 * kq + 2 * fi) * 1024u;
#define DMA_F(s) lin2_sc1<((s) % 9) * UNIT>(rws, lin16, soF + (((s) / 7) * 112 + 4 * ((s) % 7)) * 1024u, ld2k)
  DMA_F(0); DMA_F(1); DMA_F(2); DMA_F(3);
  if (w == 3) dma4b_sc1(rws, (lane & 15) * 4u, ctl + 512, EPO_OFF);
  const LB fa(kq * 4096 + lane * 16);
  v16f oA, oB;
  auto red_write = [&](const v16f& acc) {
    const int tk = lane & 31;
#pragma unroll
    for (int a4 = 0; a4 < 4; ++a4)
      *(LAS v4f*)(5 * UNIT + kq * 8192 + tk * 256 + (((8 * fi + 2 * a4 + hf) ^ (tk & 7)) * 16)) = v4f{acc[4 * a4], acc[4 * a4 + 1], acc[4 * a4 + 2], acc[4 * a4 + 3]};
  };
  auto red_sum = [&](int a) {
    const int tk = tid >> 4, c4 = tid & 15;
    const u32 ra = 5 * UNIT + tk * 256 + ((c4 ^ (tk & 7)) * 16);
    const v4f sum = (lds128f(ra) + lds128f(ra + 8192)) + (lds128f(ra + 16384) + lds128f(ra + 24576));
    const v2u hr = lds64(HRES_OFF + (a * 4 + (c4 >> 2)) * 1024 + (((c4 >> 1) & 1) * 32 + tk) * 16 + (c4 & 1) * 8);
    return v2u{pk(bflo(hr.x) + sum.x, bfhi(hr.x) + sum.y), pk(bflo(hr.y) + sum.z, bfhi(hr.y) + sum.w)};
  };
  GAS char* op = (GAS char*)out + (size_t)(t0 + (tid >> 4)) * 1024 + c * 128 + (tid & 15) * 8;
  v2u resA;
  bar_vm<6>();
  STAMP(33);
#pragma unroll
  for (int i = 0; i < 4; ++i) r_[i] = lds128(fa(i * 1024));
  DMA_F(4);
  sfor<0, 14>([&](auto ii) {
    constexpr int I = decltype(ii)::value, J = I + 1;
    if constexpr (J <= 10) bar_vm<6>(); else if constexpr (J == 11) bar_vm<4>(); else if constexpr (J == 12) bar_vm<2>(); else if constexpr (J == 13) bar_vm<0>();
    if constexpr (I == 2) {
      const v2u fl = lds64(EPO_OFF + (lane & 7) * 8);
      if ((__builtin_amdgcn_ballot_w64(fl.x == (u32)tag && fl.y == (u32)(tag >> 32)) & 0x7f) != 0x7f) {
        if (w == 3) poll_flags(rsW, ctl + 512, 7, tag, lane);
        bar();
      }
      STAMP(14);
    }
    if constexpr (I == 7) STAMP(34);
    if constexpr (I == 12) STAMP(35);
    if constexpr (J < 14) {
#pragma unroll
      for (int i = 0; i < 4; ++i) r_[4 * (J & 1) + i] = lds128(fa((J % 9) * UNIT + i * 1024));
    }
    if constexpr (I + 5 < 14) { if (!th) DMA_F(I + 5); }
    SB();
#pragma unroll
    for (int i = 0; i < 4; ++i) {
      v16f& o = I < 7 ? oA : oB;
      if (I % 7 == 0 && i == 0) { v16f z_; for (int e = 0; e < 16; ++e) z_[e] = 0.f; o = mfma32(w2f[0], r_[4 * (I & 1)], z_); }
      else o = mfma32(w2f[4 * (I % 7) + i], r_[4 * (I & 1) + i], o);
      PIN(o);
    }
    SB();
    if constexpr (I + 5 < 14) { if (th) DMA_F(I + 5); }
    if constexpr (I == 7) red_write(oA);
    if constexpr (I == 9) { resA = red_sum(0); PIN(resA); }
    if constexpr (I == 12) __hip_atomic_store((GAS u64*)op, ((u64)resA.y << 32) | resA.x, __ATOMIC_RELAXED, __HIP_MEMORY_SCOPE_AGENT);
    SB();
  });
  red_write(oB);
  bar();
  { const v2u rb = red_sum(1); __hip_atomic_store((GAS u64*)(op + 32768), ((u64)rb.y << 32) | rb.x, __ATOMIC_RELAXED, __HIP_MEMORY_SCOPE_AGENT); }
  if (c == 0 && w == 0) put_flag(rsW, ctl, tag, lane);
}

extern "C" void kernel_launch(void* const* d_in, const int* in_sizes, int n_in, void* d_out, int out_size, void* d_ws, size_t ws_size,
                              hipStream_t stream) {
  (void)hipFuncSetAttribute((const void*)fused_block, hipFuncAttributeMaxDynamicSharedMemorySize, LDS_BYTES);
  fused_block<<<256, 512, LDS_BYTES, stream>>>(d_in[0], d_in[1], d_in[2], d_in[4], d_in[5], d_out, d_ws);
}
```

```cpp
#pragma clang diagnostic ignored "-Wint-to-pointer-cast"
#include <hip/hip_runtime.h>
#include <hip/hip_bf16.h>
#include <type_traits>

typedef unsigned u32;
typedef unsigned long long u64;
typedef u32 v4u __attribute__((ext_vector_type(4)));
typedef u32 v2u __attribute__((ext_vector_type(2)));
typedef float v4f __attribute__((ext_vector_type(4)));
typedef float v2f __attribute__((ext_vector_type(2)));
typedef float v16f __attribute__((ext_vector_type(16)));
typedef short v4s __attribute__((ext_vector_type(4)));
typedef __bf16 v8bf __attribute__((ext_vector_type(8)));
typedef __bf16 v2bf __attribute__((ext_vector_type(2)));
#define LAS __attribute__((address_space(3)))
#define DEV __device__ __forceinline__

namespace {
constexpr int SEQ = 2048, DM = 512, FF = 1792;
constexpr u32 O_OFF = 65536, H_OFF = O_OFF + 2097152, F_OFF = H_OFF + 2097152, PS_OFF = F_OFF + 7340032, WS_END = PS_OFF + 524288;
constexpr u32 UNIT = 16384;
constexpr u32 XKC = 10240, WR_OFF = 81920, WSTG = 24576;
constexpr u32 HRES_OFF = 9 * UNIT;
constexpr u32 TOUCH_OFF = 155648;
constexpr u32 ST_OFF = 156160;
constexpr u32 CS_OFF = 160256;
constexpr u32 EPO_OFF = 161280;
constexpr u32 LDS_BYTES = 163840;
#define SB() __builtin_amdgcn_sched_barrier(0)
#define PIN(x) asm volatile("" : "+v"(x))

DEV v4u mk_rsrc(const void* p, u32 bytes) {
  u64 a = (u64)p;
  v4u r;
  r.x = (u32)a; r.y = (u32)(a >> 32) & 0xffffu; r.z = bytes; r.w = 0x00020000u;
  return r;
}
template <u32 OF> DEV void dma1r(v4u rs, u32 v, u32 so, u32 lds) {
  asm volatile("s_mov_b32 m0, %3\n\ts_nop 0\n\tbuffer_load_dwordx4 %0, %1, %2 offen offset:%4 lds" ::"v"(v), "s"(rs), "s"(so), "s"(lds), "n"(OF) : "memory");
}
template <u32 LI> DEV void dma3(v4u rs, u32 v, u32 so, u32 lb) {
  asm volatile("s_add_u32 m0, %3, %4\n\ts_nop 0\n\tbuffer_load_dwordx4 %0, %1, %2 offen lds\n\t"
               "buffer_load_dwordx4 %0, %1, %2 offen offset:1024 lds\n\t"
               "buffer_load_dwordx4 %0, %1, %2 offen offset:2048 lds" ::"v"(v), "s"(rs), "s"(so), "s"(lb), "n"(LI) : "memory", "scc");
}
template <u32 LI> DEV void dma2v(v4u rs, u32 v0, u32 v1, u32 so, u32 lb) {
  asm volatile("s_add_u32 m0, %4, %5\n\ts_nop 0\n\tbuffer_load_dwordx4 %0, %2, %3 offen lds\n\t"
               "buffer_load_dwordx4 %1, %2, %3 offen offset:1024 lds" ::"v"(v0), "v"(v1), "s"(rs), "s"(so), "s"(lb), "n"(LI) : "memory", "scc");
}
template <u32 LI> DEV void dma4v(v4u rs, const u32 (&v)[4], u32 so, u32 lb) {
  asm volatile("s_add_u32 m0, %6, %7\n\ts_nop 0\n\tbuffer_load_dwordx4 %0, %4, %5 offen lds\n\t"
               "buffer_load_dwordx4 %1, %4, %5 offen offset:1024 lds\n\t"
               "buffer_load_dwordx4 %2, %4, %5 offen offset:2048 lds\n\t"
               "buffer_load_dwordx4 %3, %4, %5 offen offset:3072 lds" ::"v"(v[0]), "v"(v[1]), "v"(v[2]), "v"(v[3]), "s"(rs), "s"(so), "s"(lb), "n"(LI) : "memory", "scc");
}
DEV void dma_halo(v4u rs, u32 v0, u32 v1, u32 so, u32 lds) {
  asm volatile("s_mov_b32 m0, %4\n\ts_mov_b32 exec_lo, 0xffffff00\n\tbuffer_load_dwordx4 %0, %2, %3 offen lds\n\ts_mov_b32 exec_lo, -1\n\t"
               "buffer_load_dwordx4 %1, %2, %3 offen offset:1024 lds" ::"v"(v0), "v"(v1), "s"(rs), "s"(so), "s"(lds) : "memory");
}
DEV void dma4b_sc1(v4u rs, u32 voff, u32 soff, u32 lds) {
  asm volatile("s_mov_b32 m0, %3\n\ts_nop 0\n\tbuffer_load_dword %0, %1, %2 offen sc1 lds" ::"v"(voff), "s"(rs), "s"(soff), "s"(lds) : "memory");
}
template <u32 LI> DEV void ps4_sc1(v4u rs, u32 v, u32 so, u32 lb) {
  u32 t;
  asm volatile("s_add_u32 m0, %4, %5\n\ts_add_u32 %0, %3, 3072\n\tbuffer_load_dwordx4 %1, %2, %3 offen sc1 lds\n\t"
               "buffer_load_dwordx4 %1, %2, %0 offen offset:1024 sc1 lds\n\ts_add_u32 %0, %0, 3072\n\t"
               "buffer_load_dwordx4 %1, %2, %0 offen offset:2048 sc1 lds\n\ts_add_u32 %0, %0, 3072\n\t"
               "buffer_load_dwordx4 %1, %2, %0 offen offset:3072 sc1 lds" : "=&s"(t) : "v"(v), "s"(rs), "s"(so), "s"(lb), "n"(LI) : "memory", "scc");
}
template <u32 LI> DEV void lin4_sc1(v4u rs, u32 v, u32 so, u32 lb) {
  asm volatile("s_add_u32 m0, %3, %4\n\ts_nop 0\n\tbuffer_load_dwordx4 %0, %1, %2 offen sc1 lds\n\t"
               "buffer_load_dwordx4 %0, %1, %2 offen offset:1024 sc1 lds\n\t"
               "buffer_load_dwordx4 %0, %1, %2 offen offset:2048 sc1 lds\n\t"
               "buffer_load_dwordx4 %0, %1, %2 offen offset:3072 sc1 lds" ::"v"(v), "s"(rs), "s"(so), "s"(lb), "n"(LI) : "memory", "scc");
}
DEV void touch(v4u rs, u32 voff) {
  asm volatile("s_mov_b32 m0, %2\n\ts_nop 0\n\tbuffer_load_dword %0, %1, 0 offen lds" ::"v"(voff), "s"(rs), "n"(TOUCH_OFF) : "memory");
}
DEV void bar_raw() { asm volatile("s_barrier" ::: "memory"); }
template <int N> DEV void wait_vm() { asm volatile("s_waitcnt vmcnt(%0)" ::"n"(N) : "memory"); }
DEV void bar() { asm volatile("s_waitcnt lgkmcnt(0)\n\ts_barrier" ::: "memory"); }
template <int N> DEV void bar_vm() { asm volatile("s_waitcnt vmcnt(%0) lgkmcnt(0)\n\ts_barrier" ::"n"(N) : "memory"); }

DEV v4u lds128(u32 a) { return *(LAS v4u*)a; }
DEV v2u lds64(u32 a) { return *(LAS v2u*)a; }
DEV v4f lds128f(u32 a) { return *(LAS v4f*)a; }
DEV v2f lds64f(u32 a) { return *(LAS v2f*)a; }
DEV float lds32f(u32 a) { return *(LAS float*)a; }
DEV v2u trd(u32 a) { return __builtin_bit_cast(v2u, __builtin_amdgcn_ds_read_tr16_b64_v4i16((LAS v4s*)a)); }
DEV v4u trd2(u32 a0, u32 a1) { v2u x = trd(a0), y = trd(a1); return v4u{x.x, x.y, y.x, y.y}; }
struct LB {
  u32 b[3];
  DEV explicit LB(u32 lanepart) {
#pragma unroll
    for (int i = 0; i < 3; ++i) { b[i] = lanepart + i * 65536u; asm("" : "+v"(b[i])); }
  }
  DEV u32 operator()(u32 off) const { return b[off >> 16] + (off & 0xffffu); }
};
DEV u32 opq(u32 x) { asm("" : "+v"(x)); return x; }
DEV v4f mfma16(v4u a, v4u b, v4f c) {
  return __builtin_amdgcn_mfma_f32_16x16x32_bf16(__builtin_bit_cast(v8bf, a), __builtin_bit_cast(v8bf, b), c, 0, 0, 0);
}
DEV v4f mfma16k16(v2u a, v2u b, v4f c) {
  return __builtin_amdgcn_mfma_f32_16x16x16bf16_1k(__builtin_bit_cast(v4s, a), __builtin_bit_cast(v4s, b), c, 0, 0, 0);
}
DEV v16f mfma32(v4u a, v4u b, v16f c) {
  return __builtin_amdgcn_mfma_f32_32x32x16_bf16(__builtin_bit_cast(v8bf, a), __builtin_bit_cast(v8bf, b), c, 0, 0, 0);
}
constexpr u32 ONES = 0x3f803f80u;
DEV void stat2x(u32 a, u32 b, float& sa, float& qa, float& sb, float& qb) {
  float la, ha, lb, hb;
  asm volatile("v_lshlrev_b32 %4, 16, %8\n\tv_lshlrev_b32 %6, 16, %9\n\tv_and_b32 %5, 0xffff0000, %8\n\tv_and_b32 %7, 0xffff0000, %9\n\t"
               "v_add_f32 %0, %0, %4\n\tv_add_f32 %2, %2, %6\n\tv_fmac_f32 %1, %4, %4\n\tv_fmac_f32 %3, %6, %6\n\t"
               "v_add_f32 %0, %0, %5\n\tv_add_f32 %2, %2, %7\n\tv_fmac_f32 %1, %5, %5\n\tv_fmac_f32 %3, %7, %7"
               : "+v"(sa), "+v"(qa), "+v"(sb), "+v"(qb), "=&v"(la), "=&v"(ha), "=&v"(lb), "=&v"(hb) : "v"(a), "v"(b));
}
DEV v4u silu8(const float (&a)[8], v4f c0, v4f c1, float mean, float k1, float irs) {
  float u[8], e[8];
  const float c[8] = {c0.x, c0.y, c0.z, c0.w, c1.x, c1.y, c1.z, c1.w};
  asm volatile("v_fma_f32 %0, -%16, %24, %8\n\tv_fma_f32 %1, -%17, %24, %9\n\tv_fma_f32 %2, -%18, %24, %10\n\tv_fma_f32 %3, -%19, %24, %11\n\t"
               "v_fma_f32 %4, -%20, %24, %12\n\tv_fma_f32 %5, -%21, %24, %13\n\tv_fma_f32 %6, -%22, %24, %14\n\tv_fma_f32 %7, -%23, %24, %15"
               : "=&v"(u[0]), "=&v"(u[1]), "=&v"(u[2]), "=&v"(u[3]), "=&v"(u[4]), "=&v"(u[5]), "=&v"(u[6]), "=&v"(u[7])
               : "v"(a[0]), "v"(a[1]), "v"(a[2]), "v"(a[3]), "v"(a[4]), "v"(a[5]), "v"(a[6]), "v"(a[7]),
                 "v"(c[0]), "v"(c[1]), "v"(c[2]), "v"(c[3]), "v"(c[4]), "v"(c[5]), "v"(c[6]), "v"(c[7]), "v"(mean));
#define E8(OP) OP(0, 8) OP(1, 9) OP(2, 10) OP(3, 11) OP(4, 12) OP(5, 13) OP(6, 14) OP(7, 15)
#define E_MUL(U, E) "v_mul_f32 %" #E ", %" #U ", %16\n\t"
#define E_EXP(U, E) "v_exp_f32 %" #E ", %" #E "\n\t"
#define E_FMA(U, E) "v_fma_f32 %" #E ", %" #E ", %17, %17\n\t"
#define E_RCP(U, E) "v_rcp_f32 %" #E ", %" #E "\n\t"
#define E_OUT(U, E) "v_mul_f32 %" #U ", %" #U ", %" #E "\n\t"
  asm volatile(E8(E_MUL) E8(E_EXP) E8(E_FMA) E8(E_RCP) E8(E_OUT) "s_nop 0"
               : "+v"(u[0]), "+v"(u[1]), "+v"(u[2]), "+v"(u[3]), "+v"(u[4]), "+v"(u[5]), "+v"(u[6]), "+v"(u[7]),
                 "=&v"(e[0]), "=&v"(e[1]), "=&v"(e[2]), "=&v"(e[3]), "=&v"(e[4]), "=&v"(e[5]), "=&v"(e[6]), "=&v"(e[7])
               : "v"(k1), "v"(irs));
  v2bf p0 = {(__bf16)u[0], (__bf16)u[1]}, p1 = {(__bf16)u[2], (__bf16)u[3]}, p2 = {(__bf16)u[4], (__bf16)u[5]}, p3 = {(__bf16)u[6], (__bf16)u[7]};
  return v4u{__builtin_bit_cast(u32, p0), __builtin_bit_cast(u32, p1), __builtin_bit_cast(u32, p2), __builtin_bit_cast(u32, p3)};
}
DEV v4u merge16(v2u a, v2u b) {
  auto x = __builtin_amdgcn_permlane16_swap(a.x, b.x, false, false);
  auto y = __builtin_amdgcn_permlane16_swap(a.y, b.y, false, false);
  return v4u{x[0], y[0], x[1], y[1]};
}
DEV u32 pk(float lo, float hi) { v2bf p = {(__bf16)lo, (__bf16)hi}; return __builtin_bit_cast(u32, p); }
DEV float bflo(u32 x) { return __uint_as_float(x << 16); }
DEV float bfhi(u32 x) { return __uint_as_float(x & 0xffff0000u); }
DEV u32 f2u(float x) { return __float_as_uint(x); }
DEV float u2f(u32 x) { return __uint_as_float(x); }
DEV float add16(float x) { auto r = __builtin_amdgcn_permlane16_swap(f2u(x), f2u(x), false, false); return u2f(r[0]) + u2f(r[1]); }
DEV float add32(float x) { auto r = __builtin_amdgcn_permlane32_swap(f2u(x), f2u(x), false, false); return u2f(r[0]) + u2f(r[1]); }
DEV float vadd(float a, float b) { float r; asm("v_add_f32 %0, %1, %2" : "=v"(r) : "v"(a), "v"(b)); return r; }
DEV float vmax(float a, float b) { u32 inf = 0x7f800000u; asm("" : "+s"(inf)); return __builtin_amdgcn_fmed3f(a, b, u2f(inf)); }
DEV float var_eps(float q, float mean) { float t = q * (1.f / DM); PIN(t); return vmax(__builtin_fmaf(-mean, mean, t), 0.f) + 1e-5f; }
template <int CTRL> DEV float dpp_add(float x) { return x + u2f(__builtin_amdgcn_update_dpp(0, f2u(x), CTRL, 0xf, 0xf, true)); }
DEV float red8(float x) { return dpp_add<0x141>(dpp_add<0x4E>(dpp_add<0xB1>(x))); }
DEV float bcast16(float x) { return u2f(__builtin_amdgcn_update_dpp(0, f2u(x), 0x150, 0xf, 0xf, true)); }
DEV float sel(u32 m, float t, float f) { return u2f((f2u(t) & m) | (f2u(f) & ~m)); }
template <u32 OFF> DEV void put_flag(__amdgpu_buffer_rsrc_t rs, u32 va, v2u vt) { __builtin_amdgcn_raw_buffer_store_b64(vt, rs, va + OFF, 0, 16); }
template <int SLEEP = 0> DEV void poll_flags(__amdgpu_buffer_rsrc_t rs, u32 off, int n, u64 tag, int lane) {
  const u32 a = lane < n ? off + lane * 8 : 0x40000000u;
  const u64 need = (1ull << n) - 1;
#pragma nounroll
  for (int it = 0; it < 40000; ++it) {
    asm volatile("" ::: "memory");
    const v2u v = __builtin_amdgcn_raw_buffer_load_b64(rs, a, 0, 16);
    if ((__builtin_amdgcn_ballot_w64(v.x == (u32)tag && v.y == (u32)(tag >> 32)) & need) == need) break;
    if (SLEEP) __builtin_amdgcn_s_sleep(SLEEP);
  }
  asm volatile("" ::: "memory");
}
constexpr int NE = 8, NT = 40 + 2 * NE, NEV = (NT + 3) / 4;
#define TEV(e) do { const int k_ = 4 * h + (sh >> 1), n_ = (((e) + (xcd >> 1)) & 3) * NEV + k_; if (k_ < NEV && n_ < NT) touch_k(n_); } while (0)
#define TOUCHES(s) do { if ((s) == 0) { if (!(sh & 1)) TEV(0); } if ((s) == 2) { if (sh & 1) TEV(1); } \
                        if ((s) == 4) { if (!(sh & 1)) TEV(2); } if ((s) == 5) { if (sh & 1) TEV(3); } } while (0)
constexpr int RD_ = 8;
template <int I, int N, class F> DEV void sfor(F&& f) {
  if constexpr (I < N) { f(std::integral_constant<int, I>{}); sfor<I + 1, N>(f); }
}
}

__global__ void __launch_bounds__(512, 2)
fused_block(const void* __restrict__ x, const void* __restrict__ wqkv, const void* __restrict__ wo, const void* __restrict__ w1,
            const void* __restrict__ w2, void* __restrict__ out, void* __restrict__ ws) {
  const int tid = threadIdx.x, lane = tid & 63, w = __builtin_amdgcn_readfirstlane(tid >> 6);
  const int b = blockIdx.x, xcd = b & 7, idx = b >> 3;
  const int sh = idx >> 2, q4 = idx & 3;
  const int tt = 8 * (xcd >> 1) + sh, c = 4 * (xcd & 1) + q4;
  const int rot = (sh + 2 * q4) & 7;
  const int ct = c == 3 ? 7 : c == 7 ? 3 : c;
  const int t0 = 64 * tt;
  const u32 l3 = lane >> 3, l7 = lane & 7, l4 = lane >> 4;
  const int th = w >> 2, h = w & 3;
  v4u rx, rqkv;
  const u32 ch0 = (((l7 >> 1) ^ (l4 & 1)) * 2 + (lane & 1)) * 16u, ch1 = ch0 ^ 64u;
  const u32 vWa = l3 * 3072u + ch0, vWb = l3 * 3072u + ch1 + 8 * 3072u;
  const u32 vXa = l3 * 1024u + ((l7 ^ l4) * 16u), vXb = (vXa ^ 64u) + 7168u;
  const u32 soW = (u32)h * (16 * 3072u) + (u32)c * 128u, ldW = (u32)h * 6144u;
  const u32 soX = (u32)(t0 + 16 * h) * 1024u, ldX = (u32)(2 + 2 * h) * 1024u;
#define KC(s) ((u32)(((s) + rot) & 7))
#define DMA_WA(s) dma3<WR_OFF + ((s) % 3) * WSTG>(rqkv, vWa, soW + KC(s) * (64 * 3072u), ldW)
#define DMA_WB(s) dma3<WR_OFF + ((s) % 3) * WSTG + 3072>(rqkv, vWb, soW + KC(s) * (64 * 3072u), ldW)
#define DMA_X(s) dma2v<(s) * XKC>(rx, vXa, vXb, soX + KC(s) * 128u, ldX)
  {
    u32 v0 = th ? vWb : vWa, s0 = soW + KC(0) * (64 * 3072u), l0 = ldW + th * 3072u, s1 = soX + KC(th) * 128u, l1 = ldX + th * XKC;
    u32 s2 = (u32)(tt ? t0 - 16 : 0) * 1024u + KC(w) * 128u, l2 = (u32)w * XKC, xa_ = vXa, xb_ = vXb;
    asm volatile("" : "+v"(v0), "+v"(xa_), "+v"(xb_), "+s"(s0), "+s"(l0), "+s"(s1), "+s"(l1), "+s"(s2), "+s"(l2));
    SB();
    const void *px = x, *pq = wqkv;
    asm volatile("" : "+s"(px), "+s"(pq) : "s"(wo), "s"(w1), "s"(w2), "s"(out), "s"(ws));
    rx = mk_rsrc(px, SEQ * DM * 2); rqkv = mk_rsrc(pq, DM * 3 * DM * 2);
    dma3<WR_OFF>(rqkv, v0, s0, l0);
    dma2v<0>(rx, xa_, xb_, s1, l1);
    dma_halo(rx, xa_, xb_, s2, l2);
  }

  const v4u rwo = mk_rsrc(wo, DM * DM * 2);
  const v4u rw1 = mk_rsrc(w1, ct < 7 ? DM * FF * 2 : 0), rw2 = mk_rsrc(w2, FF * DM * 2), rws = mk_rsrc(ws, WS_END);
  const int tok = lane & 15, g = lane >> 4, q = (lane & 15) >> 2, p = lane & 3, hf = lane >> 5, blk = (lane >> 4) & 1;
  const __amdgpu_buffer_rsrc_t rsW = __builtin_amdgcn_make_buffer_rsrc(ws, 0, WS_END, 0x00020000);
  const __amdgpu_buffer_rsrc_t rsF = __builtin_amdgcn_make_buffer_rsrc(ws, 0, ct < 7 ? WS_END : 0, 0x00020000);
  const u32 ctl = (u32)tt * 1024u;
  const u32 lin16 = opq(lane * 16u);
  const u32 ld2k = (u32)w * 2048u;
  u32 vW1[2];
#pragma unroll
  for (int i = 0; i < 2; ++i) vW1[i] = hf * 3584u + (((((lane & 31) >> 2) ^ (2 * i + hf)) * 4 + (lane & 3)) * 16u) + i * 6144u;
  const u32 soW1 = (u32)w * (4 * 3584u) + (u32)ct * 512u;
#define DMA_W1(u, unit) dma2v<(unit) * UNIT>(rw1, vW1[0], vW1[1], soW1 + (u) * (32 * 3584u), ld2k)
  const int co = ct < 7 ? ct : 6;
#define PC(v) ((v) == 0 ? (u32)co : (u32)((v) - 1 + ((v) - 1 >= co)))
  auto w2line = [&](int j) {
    const int u = (int)(0xEDC654B3A98102ull >> (4 * (j >> 1))) & 15, v = u & 7;
    return (PC(v) * 256u + (u >> 3) * 128u + (j & 1) * 64u + lane) * 1024u + (u32)c * 128u;
  };
  const int rslot = (c - rot) & 7;
  const u32 xra = (u32)rslot * XKC + (16 + 32 * th + tok) * 128 + (((2 * h + (g >> 1)) ^ (tok >> 1)) * 16) + (g & 1) * 8;
  v2u xres[2], epo;
#define XRES() do { xres[0] = lds64(xra); xres[1] = lds64(xra + 2048); } while (0)

  if (!th) {
    __builtin_amdgcn_s_setprio(2);
    DMA_WA(1); DMA_WB(1);
    if (h == 3) dma4b_sc1(rws, (lane & 1) * 4u, (u32)tt * 1024u, EPO_OFF);
    const u32 vWo0 = l3 * 1024u + ch0, vWo1 = l3 * 1024u + ch1 + 7168u;
    const u32 soWo = (u32)h * 16384u + (u32)c * 128u, ldo = (u32)h * 2048u;
#define DMA_WO(pp) dma2v<(pp) * 8192>(rwo, vWo0, vWo1, soWo + (pp) * 65536u, ldo)
#define DMA_W1H(u, unit) do { dma2v<(unit) * UNIT>(rw1, vW1[0], vW1[1], soW1 + (u) * (32 * 3584u), ld2k); \
                              dma2v<(unit) * UNIT + 8192>(rw1, vW1[0], vW1[1], soW1 + 16 * 3584 + (u) * (32 * 3584u), ld2k); } while (0)
    auto touch_k = [&](int k) {
      if (k < 8) touch(rwo, (u32)(k * 64 + lane) * 1024u + (u32)c * 128u);
      else if (k < 40) touch(rw1, (u32)((k - 8) * 16 + (lane >> 2)) * 3584u + (u32)ct * 512u + (lane & 3) * 128u);
      else touch(rw2, w2line(k - 40));
    };
    const u32 xl = (16 + 16 * h + tok) * 128, x0l = tok ? xl : 0;
    const LB xb0(xl + ((g ^ (tok >> 1)) * 16)), xb1(xl + (((4 + g) ^ (tok >> 1)) * 16));
    const LB xz0(x0l + ((g ^ (tok >> 1)) * 16)), xz1(x0l + (((4 + g) ^ (tok >> 1)) * 16));
    const u32 shl = opq((l3 & 1) * XKC + (4 * h + g) * 128 + l7 * 16);
    float sa = 0, qa = 0, sb = 0, qb = 0;
    v4f G = {0, 0, 0, 0};
    auto fin = [&](float s_, float q_, u32 a) {
      float mean = s_ * (1.f / DM); PIN(mean);
      const float rs = __builtin_amdgcn_rsqf(var_eps(q_, mean));
      *(LAS float*)a = mean; *(LAS float*)(a + 320) = rs;
    };
    v4u gr[4];
#define GRD(s) do { gr[0] = lds128(xz0((s) * XKC)); gr[1] = lds128(xb0((s) * XKC)); gr[2] = lds128(xz1((s) * XKC)); gr[3] = lds128(xb1((s) * XKC)); } while (0)
#define GMM() do { G = mfma16(gr[0], gr[1], G); G = mfma16(gr[2], gr[3], G); PIN(G); } while (0)
    v4u hx[4];
#define S2(i, d) do { const u32 a_ = hx[i][d], b_ = hx[(i) + 1][d]; stat2x(a_, b_, sa, qa, sb, qb); } while (0)
    const u32 r0a = opq(g * XKC + (16 + 16 * h) * 128 + tok * 8);
    float s0a = 0, q0a = 0, s0b = 0, q0b = 0;
    bar_vm<6>();
    GRD(0);
#pragma unroll
    for (int i = 0; i < 4; ++i) hx[i] = lds128(shl + 2 * i * XKC);
    DMA_X(2); DMA_WA(2); GMM(); DMA_WB(2); TOUCHES(0);
    bar_vm<8>();
    GRD(1); epo = lds64(EPO_OFF);
    DMA_X(3); S2(0, 0); DMA_WA(3); GMM(); S2(0, 1); DMA_WB(3); TOUCHES(1);
    bar_vm<8>();
    GRD(2);
    DMA_X(4); S2(0, 2); DMA_WA(4); GMM(); S2(0, 3); DMA_WB(4); TOUCHES(2);
    bar_vm<8>();
    GRD(3);
    DMA_X(5); S2(2, 0); DMA_WA(5); GMM(); S2(2, 1); DMA_WB(5); TOUCHES(3);
    bar_vm<8>();
    GRD(4); XRES();
    const v2u r0 = lds64(r0a);
    DMA_X(6); S2(2, 2); DMA_WA(6); GMM(); S2(2, 3); DMA_WB(6); TOUCHES(4);
    bar_vm<8>();
    GRD(5);
    DMA_X(7);
    { const u32 a = r0.x, b2 = r0.y; stat2x(a, b2, s0a, q0a, s0b, q0b); }
    DMA_WA(7); GMM();
    { float s_ = vadd(sa, sb); PIN(s_); s_ = dpp_add<0x140>(red8(s_)); PIN(s_); float q_ = vadd(qa, qb); PIN(q_); q_ = dpp_add<0x140>(red8(q_)); PIN(q_); fin(s_, q_, ST_OFF + (4 * h + g) * 4); }
    DMA_WB(7); TOUCHES(5);
    bar_vm<8>();
    GRD(6);
    DMA_WO(0); DMA_WO(1); GMM(); DMA_WO(2); DMA_WO(3);
    bar_vm<8>();
    GRD(7);
    {
      const v2u r1 = lds64(r0a + 4 * XKC);
      if (rslot >= 4) XRES();
      GMM();
      const u32 a = r1.x, b2 = r1.y;
      stat2x(a, b2, s0a, q0a, s0b, q0b);
      float q0 = vadd(q0a, q0b); PIN(q0);
      q0 = add32(add16(dpp_add<0x140>(red8(q0))));
      auto r = __builtin_amdgcn_permlane16_swap(f2u(G.x), f2u(G.x), false, false);
      auto r2 = __builtin_amdgcn_permlane32_swap(r[0], r[0], false, false);
      const float dg = (tok & 2) ? ((tok & 1) ? G.w : G.z) : ((tok & 1) ? G.y : G.x);
      fin(u2f(r2[0]), tok ? dg : q0, g == (tok >> 2) ? ST_OFF + (16 + 16 * h + tok) * 4 : ST_OFF + 1024);
    }
    bar();
    DMA_WO(4); DMA_WO(5); DMA_WO(6); DMA_WO(7); DMA_W1H(0, 5); DMA_W1H(1, 8); DMA_W1H(2, 4);
    wait_vm<0>();
  } else {
    __builtin_amdgcn_s_setprio(0);
    if (h == 0) *(LAS v4u*)(l3 * XKC + l7 * 16) = v4u{ONES, ONES, ONES, ONES};
    if (h == 1) {
      u64 pc; int len;
      asm volatile("s_getpc_b64 %0\n.Lfb_pc:\n\ts_mov_b32 %1, .Lfb_end-.Lfb_pc" : "=s"(pc), "=s"(len));
      touch(mk_rsrc((const void*)pc, len > 0 ? len : 0), (u32)(idx * 8 + (lane & 7)) * 128u);
    }
    v4f aK[5], aV[5], aQ[5];
#pragma unroll
    for (int i = 0; i < 5; ++i) { aK[i] = v4f{0, 0, 0, 0}; aV[i] = v4f{0, 0, 0, 0}; aQ[i] = v4f{0, 0, 0, 0}; }
    const LB xa0(tok * 128 + ((g ^ (tok >> 1)) * 16)), xa1(tok * 128 + (((4 + g) ^ (tok >> 1)) * 16));
    const LB wa(g * 3072 + q * 128 + ((h ^ ((q >> 1) | ((g & 1) << 1))) * 32) + 8 * p);
    const int d0 = tok - 4 * g - (tok & 1);
    const bool odd = tok & 1, cur0 = d0 >= 0, cur1 = d0 >= 2;
    u32 mOdd = odd ? ~0u : 0u, mC0 = cur0 ? ~0u : 0u, mC1 = cur1 ? ~0u : 0u;
    PIN(mOdd); PIN(mC0); PIN(mC1);
    u32 selP[4];
    {
      const u32 s0 = odd ? 0x01000c0cu : 0x0c0c0100u, s1 = odd ? 0x03020c0cu : 0x0c0c0302u, z = 0x0c0c0c0cu;
      selP[0] = cur0 ? z : s0; selP[1] = cur1 ? z : s1; selP[2] = cur0 ? s0 : z; selP[3] = cur1 ? s1 : z;
      PIN(selP[0]); PIN(selP[1]); PIN(selP[2]); PIN(selP[3]);
    }
    u32 stT = ST_OFF + tok * 4, stG = ST_OFF + 4 * g * 4;
    u32 stK0 = ST_OFF + 320 + (4 * g + (tok & 1) + (cur0 ? 16 : 0)) * 4;
    u32 stK1 = ST_OFF + 320 + (4 * g + (tok & 1) + 2 + (cur1 ? 16 : 0)) * 4;
    const bool first = tt == 0;
    float keep0 = (first && !cur0) ? 0.f : 1.f, keep1 = (first && !cur1) ? 0.f : 1.f;
    float bias0 = (first && !cur0) ? -1.44269504e9f : 0.f, bias1 = (first && !cur1) ? -1.44269504e9f : 0.f;
    u32 ooff = O_OFF + (u32)(((tt * 8 + c) * 4 + (g & 1)) * 2 + (h >> 1)) * 1024u + (u32)((2 * (h & 1) + (g >> 1)) * 16 + tok) * 16u;
    PIN(stT); PIN(stG); PIN(stK0); PIN(stK1); PIN(keep0); PIN(keep1); PIN(bias0); PIN(bias1); PIN(ooff);
    float mt[5], rq[4], rho[4][2], fac[4][2];
    v4f m4[5];
    v4u fx[2][5], fq[2], fk[2], fv[2];
    bar_vm<0>();
    fk[0] = trd2(wa(WR_OFF + 1024), wa(WR_OFF + 1024 + 512));
#pragma unroll
    for (int i = 0; i < 5; ++i) fx[0][i] = lds128(xa0(i * 2048));
    fq[0] = trd2(wa(WR_OFF), wa(WR_OFF + 512));
    fv[0] = trd2(wa(WR_OFF + 2048), wa(WR_OFF + 2048 + 512));
    sfor<0, 16>([&](auto kk) {
      constexpr int K = decltype(kk)::value, N = K + 1, sN = N >> 1, ksN = N & 1, A = K & 1, B = N & 1;
      if constexpr (K & 1) { bar(); }
      constexpr u32 xb = sN * XKC, wb = WR_OFF + (sN % 3) * WSTG + ksN * 12288;
      const LB& xa = ksN ? xa1 : xa0;
      constexpr bool R = K < 15;
      SB();
      aK[0] = mfma16(fk[A], fx[A][0], aK[0]); if constexpr (R) fk[B] = trd2(wa(wb + 1024), wa(wb + 1024 + 512));
      if constexpr (K == 15) { for (int i = 0; i < 4; ++i) { rq[i] = lds32f(stT + 320 + 64 * (i + 1)); } }
      SB();
      aK[1] = mfma16(fk[A], fx[A][1], aK[1]); if constexpr (R) fx[B][0] = lds128(xa(xb));
      if constexpr (K == 15) { for (int i = 0; i < 4; ++i) rho[i][0] = lds32f(stK0 + 64 * i); }
      SB();
      aK[2] = mfma16(fk[A], fx[A][2], aK[2]); if constexpr (R) fx[B][1] = lds128(xa(xb + 2048));
      if constexpr (K == 15) { for (int i = 0; i < 4; ++i) rho[i][1] = lds32f(stK1 + 64 * i); }
      SB();
      aK[3] = mfma16(fk[A], fx[A][3], aK[3]); if constexpr (R) fx[B][2] = lds128(xa(xb + 4096));
      if constexpr (K == 15) { for (int i = 0; i < 5; ++i) mt[i] = lds32f(stT + 64 * i); }
      SB();
      aK[4] = mfma16(fk[A], fx[A][4], aK[4]); if constexpr (R) fx[B][3] = lds128(xa(xb + 6144));
      if constexpr (K == 15) { m4[0] = lds128f(stG); m4[1] = lds128f(stG + 64); }
      SB();
      aQ[0] = mfma16(fq[A], fx[A][0], aQ[0]); if constexpr (R) fx[B][4] = lds128(xa(xb + 8192));
      if constexpr (K == 15) { m4[2] = lds128f(stG + 128); m4[3] = lds128f(stG + 192); }
      SB();
      aQ[1] = mfma16(fq[A], fx[A][1], aQ[1]); if constexpr (R) fq[B] = trd2(wa(wb), wa(wb + 512));
      if constexpr (K == 15) m4[4] = lds128f(stG + 256);
      SB();
      aQ[2] = mfma16(fq[A], fx[A][2], aQ[2]); if constexpr (R) fv[B] = trd2(wa(wb + 2048), wa(wb + 2048 + 512)); SB();
      aQ[3] = mfma16(fq[A], fx[A][3], aQ[3]);
      if constexpr (K == 7) { XRES(); epo = lds64(EPO_OFF); }
      if constexpr (K == 13) { if (rslot >= 4) XRES(); }
      SB();
      aQ[4] = mfma16(fq[A], fx[A][4], aQ[4]);
      if constexpr (K == 15) { rq[0] *= 0.25f * 1.44269504f; PIN(rq[0]); rq[1] *= 0.25f * 1.44269504f; PIN(rq[1]); }
      SB();
      aV[0] = mfma16(fx[A][0], fv[A], aV[0]);
      if constexpr (K == 15) { rq[2] *= 0.25f * 1.44269504f; PIN(rq[2]); rq[3] *= 0.25f * 1.44269504f; PIN(rq[3]); }
      SB();
      aV[1] = mfma16(fx[A][1], fv[A], aV[1]);
      if constexpr (K == 15) { fac[0][0] = rho[0][0] * rq[0] * keep0; PIN(fac[0][0]); fac[0][1] = rho[0][1] * rq[0] * keep1; PIN(fac[0][1]); }
      SB();
      aV[2] = mfma16(fx[A][2], fv[A], aV[2]);
      if constexpr (K == 15) { fac[1][0] = rho[1][0] * rq[1]; PIN(fac[1][0]); fac[1][1] = rho[1][1] * rq[1]; PIN(fac[1][1]); }
      SB();
      aV[3] = mfma16(fx[A][3], fv[A], aV[3]);
      if constexpr (K == 15) { fac[2][0] = rho[2][0] * rq[2]; PIN(fac[2][0]); fac[2][1] = rho[2][1] * rq[2]; PIN(fac[2][1]); }
      SB();
      aV[4] = mfma16(fx[A][4], fv[A], aV[4]);
      if constexpr (K == 15) { fac[3][0] = rho[3][0] * rq[3]; PIN(fac[3][0]); fac[3][1] = rho[3][1] * rq[3]; PIN(fac[3][1]); }
      if constexpr (K == 13) { PIN(aK[4]); PIN(aQ[4]); PIN(aV[4]); }
      SB();
    });
    SB();
    const v4f ck = {bcast16(aK[0].x), bcast16(aK[0].y), bcast16(aK[0].z), bcast16(aK[0].w)};
    const v4f cq = {bcast16(aQ[0].x), bcast16(aQ[0].y), bcast16(aQ[0].z), bcast16(aQ[0].w)};
    v2u Kp[5], Vp[5], Qp[4];
#pragma unroll
    for (int i = 0; i < 5; ++i) {
      const v4f k = aK[i] - ck * mt[i];
      Kp[i] = v2u{pk(k.x, k.y), pk(k.z, k.w)};
      if (i > 0) { const v4f qv = aQ[i] - cq * mt[i]; Qp[i - 1] = v2u{pk(qv.x, qv.y), pk(qv.z, qv.w)}; }
    }
    const v4f z = {0, 0, 0, 0};
    v4f sp[4], sc[4];
#pragma unroll
    for (int i = 0; i < 4; ++i) { sp[i] = mfma16k16(Kp[i], Qp[i], z); sc[i] = mfma16k16(Kp[i + 1], Qp[i], z); }
    float cv;
    {
      auto r = __builtin_amdgcn_permlane16_swap(f2u(aV[0].x), f2u(aV[0].x), false, false);
      auto r2 = __builtin_amdgcn_permlane32_swap(r[0], r[0], false, false);
      cv = u2f(r2[0]);
    }
#pragma unroll
    for (int i = 0; i < 5; ++i) { const v4f v = aV[i] - m4[i] * cv; Vp[i] = v2u{pk(v.x, v.y), pk(v.z, v.w)}; }
    float e0[4], e1[4], mx[4];
#pragma unroll
    for (int i = 0; i < 4; ++i) {
      e0[i] = sel(mC0, sel(mOdd, sc[i].y, sc[i].x), sel(mOdd, sp[i].y, sp[i].x)) * fac[i][0];
      e1[i] = sel(mC1, sel(mOdd, sc[i].w, sc[i].z), sel(mOdd, sp[i].w, sp[i].z)) * fac[i][1];
      if (i == 0) { e0[0] += bias0; e1[0] += bias1; }
      mx[i] = vmax(e0[i], e1[i]);
    }
#define RED4(v, OP, POST) do { \
      auto r01 = __builtin_amdgcn_permlane16_swap(f2u(v[0]), f2u(v[1]), false, false); \
      auto r23 = __builtin_amdgcn_permlane16_swap(f2u(v[2]), f2u(v[3]), false, false); \
      auto rr = __builtin_amdgcn_permlane32_swap(f2u(OP(u2f(r01[0]), u2f(r01[1]))), f2u(OP(u2f(r23[0]), u2f(r23[1]))), false, false); \
      const float t_ = POST(OP(u2f(rr[0]), u2f(rr[1])));     \
      auto g_ = __builtin_amdgcn_permlane32_swap(f2u(t_), f2u(t_), false, false); \
      auto ga = __builtin_amdgcn_permlane16_swap(g_[0], g_[0], false, false); \
      auto gb = __builtin_amdgcn_permlane16_swap(g_[1], g_[1], false, false); \
      v[0] = u2f(ga[0]); v[1] = u2f(ga[1]); v[2] = u2f(gb[0]); v[3] = u2f(gb[1]); } while (0)
#define OP_ADD(a, b) ((a) + (b))
#define OP_ID(a) (a)
    RED4(mx, vmax, OP_ID);
    v4f o4[4];
    float sm[4];
#pragma unroll
    for (int i = 0; i < 4; ++i) {
      e0[i] = __builtin_amdgcn_exp2f(e0[i] - mx[i]); e1[i] = __builtin_amdgcn_exp2f(e1[i] - mx[i]);
      const u32 pp = pk(e0[i] * rho[i][0], e1[i] * rho[i][1]);
      const v4u P = {__builtin_amdgcn_perm(0u, pp, selP[0]), __builtin_amdgcn_perm(0u, pp, selP[1]), __builtin_amdgcn_perm(0u, pp, selP[2]), __builtin_amdgcn_perm(0u, pp, selP[3])};
      o4[i] = mfma16(v4u{Vp[i].x, Vp[i].y, Vp[i + 1].x, Vp[i + 1].y}, P, z);
      sm[i] = e0[i] + e1[i];
    }
    RED4(sm, OP_ADD, __builtin_amdgcn_rcpf);
    v2u op[4];
#pragma unroll
    for (int i = 0; i < 4; ++i) { const v4f o = o4[i] * sm[i]; op[i] = v2u{pk(o.x, o.y), pk(o.z, o.w)}; }
    __builtin_amdgcn_raw_buffer_store_b128(merge16(op[0], op[1]), rsW, ooff, 0, 16);
    __builtin_amdgcn_raw_buffer_store_b128(merge16(op[2], op[3]), rsW, ooff + 4096, 0, 16);
    __builtin_amdgcn_s_setprio(2);
  }
  u64 tag;
  {
    const u64 ep = ((u64)(u32)__builtin_amdgcn_readfirstlane(epo.y) << 32) | (u32)__builtin_amdgcn_readfirstlane(epo.x);
    tag = ep * 0x9E3779B97F4A7C15ull + 0x632BE59BD9B4E019ull;
  }
  u32 fla = lane == 0 ? ctl + ct * 8 : 0x40000000u;
  v2u flt = {(u32)tag, (u32)(tag >> 32)};
  PIN(fla); PIN(flt);
  bar_raw();
  v4u wof[16], w1f[32];
  v4f csa = {0, 0, 0, 0};
  const u32 selv = ((tok ^ g) & 1) ? 0u : ONES;
  const v4u selB = {selv, selv, selv, selv};
  const LB w1a((8 * hf + q) * 512 + ((w ^ q) * 64) + 32 * blk + 8 * p);
  const LB lin(lane * 16);
#define RD_W1(u, unit) do { w1f[2 * (u)] = trd2(w1a((unit) * UNIT), w1a((unit) * UNIT + 2048)); \
                            w1f[2 * (u) + 1] = trd2(w1a((unit) * UNIT + 8192), w1a((unit) * UNIT + 8192 + 2048)); } while (0)
#define CS_W1(u) do { csa = mfma16(w1f[2 * (u)], selB, csa); csa = mfma16(w1f[2 * (u) + 1], selB, csa); PIN(csa); } while (0)
  RD_W1(0, 5); RD_W1(1, 8); RD_W1(2, 4);
  if (!th) { DMA_W1H(3, 6); DMA_W1H(4, 7); }
  {
    const u32 wa = (8 * g + q) * 128 + ((h ^ ((q >> 1) | ((g & 1) << 1))) * 32) + 8 * p;
#pragma unroll
    for (int ks = 0; ks < 8; ++ks) wof[ks] = trd2(wa + ks * 4096, wa + ks * 4096 + 512);
    if (th) wait_vm<0>();
    bar();
    if (w == 0) put_flag<128>(rsW, fla, flt);
#pragma unroll
    for (int ks = 8; ks < 16; ++ks) wof[ks] = trd2(wa + ks * 4096, wa + ks * 4096 + 512);
  }
  u32 hoff = lin16 + H_OFF + (u32)((tt * 2 + th) * 32 + 4 * c + h) * 1024u;
  u32 poff = PS_OFF + (u32)(tt * 32 + 4 * c + h) * 512u + th * 256u + lane * 8u + (lane >= 32 ? 0x40000000u : 0u);
  v4f xr0 = {bflo(xres[0].x), bfhi(xres[0].x), bflo(xres[0].y), bfhi(xres[0].y)}, xr1 = {bflo(xres[1].x), bfhi(xres[1].x), bflo(xres[1].y), bfhi(xres[1].y)};
  u32 oa = th * 4096 + lane * 16;
  PIN(hoff); PIN(poff); PIN(xr0); PIN(xr1); PIN(oa);
  DMA_W1(5, 5); DMA_W1(6, 8); DMA_W1(7, 4);
  if (w == 7) poll_flags(rsW, ctl + 128, 8, tag, lane);
  bar_vm<0>();
  {
    const u32 so = O_OFF + (u32)tt * 65536u + (u32)w * 8192u;
    lin4_sc1<0>(rws, lin16, so, (u32)w * 8192u);
    RD_W1(3, 6); RD_W1(4, 7);
    if (th) bar();
    lin4_sc1<4096>(rws, lin16, so + 4096u, (u32)w * 8192u);
    if (!th) bar_vm<0>();
  }
  {
    v4f a0 = xr0, a1 = xr1;
    v4u r[8];
#define O_AD(i) (((i) >> 2) * 8192 + (((i) >> 1) & 1) * 1024 + ((i) & 1) * 2048)
#pragma unroll
    for (int i = 0; i < 8; ++i) r[i] = lds128(oa + O_AD(i));
    SB();
    sfor<0, 32>([&](auto ii) {
      constexpr int I = decltype(ii)::value;
      if constexpr (I == 12) {
        if (th) wait_vm<0>();
        bar();
#pragma unroll
        for (int i = 0; i < 4; ++i) r[i] = lds128(oa + O_AD(16 + i));
        SB();
      }
      if constexpr (I & 1) { a1 = mfma16(wof[I >> 1], r[I & 7], a1); PIN(a1); } else { a0 = mfma16(wof[I >> 1], r[I & 7], a0); PIN(a0); }
      if constexpr (I < 8 || (I >= 12 && I + 8 < 32)) r[I & 7] = lds128(oa + O_AD(I + 8));
      if constexpr (I == 6) RD_W1(5, 5);
      if constexpr (I == 8) RD_W1(6, 8);
      if constexpr (I == 9) RD_W1(7, 4);
      if constexpr (I == 11) { if (!th) DMA_W1H(8, 6); }
      if constexpr (I == 12) { if (!th) DMA_W1(9, 7); }
      if constexpr (I == 14) { if (th) DMA_W1(9, 7); }
      if constexpr (I == 17) { if (!th) DMA_W1(10, 5); }
      if constexpr (I == 19) { if (th) DMA_W1(10, 5); }
      if constexpr (I == 22) { if (!th) DMA_W1(11, 8); }
      if constexpr (I == 24) { if (th) DMA_W1(11, 8); }
      SB();
    });
    const v2u p0 = {pk(a0.x, a0.y), pk(a0.z, a0.w)}, p1 = {pk(a1.x, a1.y), pk(a1.z, a1.w)};
    const v4u h16 = merge16(p0, p1);
    __builtin_amdgcn_raw_buffer_store_b128(h16, rsW, hoff, 0, 16);
    SB();
    a0 = v4f{bflo(p0.x), bfhi(p0.x), bflo(p0.y), bfhi(p0.y)}; a1 = v4f{bflo(p1.x), bfhi(p1.x), bflo(p1.y), bfhi(p1.y)};
    const float s0 = (a0.x + a0.y) + (a0.z + a0.w), s1 = (a1.x + a1.y) + (a1.z + a1.w);
    const float q0 = (a0.x * a0.x + a0.y * a0.y) + (a0.z * a0.z + a0.w * a0.w), q1 = (a1.x * a1.x + a1.y * a1.y) + (a1.z * a1.z + a1.w * a1.w);
    auto rs_ = __builtin_amdgcn_permlane16_swap(f2u(s0), f2u(s1), false, false);
    auto rq_ = __builtin_amdgcn_permlane16_swap(f2u(q0), f2u(q1), false, false);
    const v2u ps = {f2u(add32(u2f(rs_[0]) + u2f(rs_[1]))), f2u(add32(u2f(rq_[0]) + u2f(rq_[1])))};
    __builtin_amdgcn_raw_buffer_store_b64(ps, rsW, poff, 0, 16);
    *(LAS v4u*)(HRES_OFF + w * 1024 + lane * 16) = h16;
  }
  bar();
  DMA_W1(12, 0); DMA_W1(13, 1);
  CS_W1(0); CS_W1(1); CS_W1(2); CS_W1(3); CS_W1(4); CS_W1(5); CS_W1(6);
  bar_vm<4>();
  if (w == 0) {
    put_flag<256>(rsW, fla, flt);
    if (ct == 0) put_flag<0>(rsW, fla, flt);
  }
  RD_W1(8, 6); RD_W1(9, 7); RD_W1(10, 5); RD_W1(11, 8);
  DMA_W1(14, 2); DMA_W1(15, 3);
  CS_W1(7); CS_W1(8); CS_W1(9); CS_W1(10); CS_W1(11);
  u32 vW2[4];
#pragma unroll
  for (int i = 0; i < 4; ++i) vW2[i] = opq(l3 * 1024u + ((l7 ^ ((l4 & 1) << 2)) * 16u) + i * 7168u);
  const u32 soW2 = (u32)(32 * h) * 1024u + (u32)c * 128u, ld4k = (u32)h * 4096u;
#define DMA_W2(G, v, unit) dma4v<(unit) * UNIT>(rw2, vW2, soW2 + ((G) * 128 + PC(v) * 256) * 1024u, ld4k)
  if (w == 7) poll_flags(rsW, ctl + 256, 8, tag, lane);
  bar_vm<0>();
  constexpr u32 HT = 5 * UNIT;
  {
    const u32 so = H_OFF + (u32)tt * 65536u + (u32)w * 4096u;
    lin4_sc1<HT>(rws, lin16, so, (u32)w * 4096u);
    if (th) ps4_sc1<4 * UNIT>(rws, l3 * 512u + l7 * 16u, PS_OFF + (u32)tt * 16384u + (u32)h * 128u, ld4k);
  }
  RD_W1(12, 0); RD_W1(13, 1); RD_W1(14, 2); RD_W1(15, 3);
  CS_W1(12); CS_W1(13); CS_W1(14); CS_W1(15);
  v4f cs4[4];
  *(LAS v4f*)(CS_OFF + w * 128 + (tok & 1) * 64 + g * 16) = csa;
#pragma unroll
  for (int j = 0; j < 4; ++j) cs4[j] = lds128f(CS_OFF + w * 128 + j * 32 + hf * 16);
  if (th) {
    const u32 so = H_OFF + (u32)tt * 65536u + 32768u + (u32)h * 8192u;
    lin4_sc1<HT + 32768>(rws, lin16, so, (u32)h * 8192u);
    wait_vm<8>();
  } else wait_vm<0>();
  bar();
  v16f acc;
  float mnA, k1A, irA, mnB, k1B, irB;
  v4u w2f[28];
  const u32 w2l = h * 4096 + (4 * hf + q) * 128 + ((q >> 1) * 64) + 32 * blk + 8 * p;
  const u32 w2a[2] = {opq(w2l + th * 3 * UNIT), opq((w2l ^ 64) + th * 3 * UNIT)};
  const u32 w2d = th ? -3 * UNIT : 6 * UNIT;
#define RD_W2F(v, i, off) w2f[4 * (v) + (i)] = trd2(w2a[(i) & 1] + (off) + ((i) >> 1) * 2048, w2a[(i) & 1] + (off) + ((i) >> 1) * 2048 + 1024)
#define RD_W2(v, off) do { RD_W2F(v, 0, off); RD_W2F(v, 1, off); RD_W2F(v, 2, off); RD_W2F(v, 3, off); } while (0)
  v4u f0_, f1_;
#define EPI(mean, k1, irs) do { \
    const float a0_[8] = {acc[0], acc[1], acc[2], acc[3], acc[4], acc[5], acc[6], acc[7]}; \
    const float a1_[8] = {acc[8], acc[9], acc[10], acc[11], acc[12], acc[13], acc[14], acc[15]}; \
    f0_ = silu8(a0_, cs4[0], cs4[1], mean, k1, irs); f1_ = silu8(a1_, cs4[2], cs4[3], mean, k1, irs); } while (0)
#define ST_F(off) do { __builtin_amdgcn_raw_buffer_store_b128(f0_, rsF, foff + (off), 0, 16); \
                       __builtin_amdgcn_raw_buffer_store_b128(f1_, rsF, foff + (off) + 1024, 0, 16); } while (0)
  const u32 foff = opq(lin16 + F_OFF + (u32)(tt * 2 * 112 + 16 * ct + 2 * w) * 1024u);
  v4u r_[8];
#define P3_PRE(a) do { _Pragma("unroll") for (int i = 0; i < 8; ++i) r_[i] = lds128(lin(HT + (a) * 32768 + i * 1024)); } while (0)
  P3_PRE(0);
#define W2SRC(G, v) (soW2 + ((G) * 128 + PC(v) * 256) * 1024u)
  if (th) {
    const u32 pa = opq(4 * UNIT + ld4k + g * 1024 + tok * 8);
    {
      const u32 so = H_OFF + (u32)tt * 65536u + 32768u + (u32)h * 8192u;
      lin4_sc1<HT + 32768 + 4096>(rws, lin16, so + 4096u, (u32)h * 8192u);
    }
    wait_vm<8>();
    v2f t[8];
#pragma unroll
    for (int j = 0; j < 8; ++j) t[j] = lds64f(pa + j * 128);
    SB();
    const float s_ = vadd(vadd(vadd(t[0].x, t[1].x), vadd(t[2].x, t[3].x)), vadd(vadd(t[4].x, t[5].x), vadd(t[6].x, t[7].x)));
    const float q_ = vadd(vadd(vadd(t[0].y, t[1].y), vadd(t[2].y, t[3].y)), vadd(vadd(t[4].y, t[5].y), vadd(t[6].y, t[7].y)));
    float s2 = add16(s_); PIN(s2); s2 = add32(s2); PIN(s2);
    float q2 = add16(q_); PIN(q2); q2 = add32(q2); PIN(q2);
    float mean = s2 * (1.f / DM); PIN(mean);
    float ve = var_eps(q2, mean); PIN(ve);
    const float rs = __builtin_amdgcn_rsqf(ve);
    float k1 = rs * -1.44269504f; PIN(k1);
    const u32 a = ST_OFF + (16 * h + tok) * 4;
    *(LAS float*)a = mean; *(LAS float*)(a + 256) = k1; *(LAS float*)(a + 512) = ve * rs;
    DMA_W2(0, 2, 2);
    wait_vm<4>();
    if (2 * NE + 8 * h + sh < 28) touch(rw2, w2line(2 * NE + 8 * h + sh));
    bar();
  }
#define W2P(i, so, ld) dma1r<(i) * 1024>(rw2, vW2[i], so, ld)
#define P3_LOOP(a, ...) do { \
    __builtin_amdgcn_s_setprio(3); \
    SB(); \
    sfor<0, 32>([&](auto ii) { constexpr int I = decltype(ii)::value; \
      if constexpr (I == 0) { v16f z_; _Pragma("unroll") for (int i = 0; i < 16; ++i) z_[i] = 0.f; acc = mfma32(w1f[0], r_[0], z_); } \
      else acc = mfma32(w1f[I], r_[I & 7], acc); \
      PIN(acc); \
      if constexpr (I + 8 < 32) r_[I & 7] = lds128(lin(HT + (a) * 32768 + (I + 8) * 1024)); \
      __VA_ARGS__ \
      SB(); }); \
    asm volatile("s_nop 9");        \
    __builtin_amdgcn_s_setprio(0); } while (0)
  {
    u32 sA = W2SRC(th, 0), sB = W2SRC(th, 1), lA = (th ? 3 * UNIT : 0) + ld4k, lB = lA + UNIT;
    asm volatile("" : "+s"(sA), "+s"(sB), "+s"(lA), "+s"(lB));
    P3_LOOP(0,
      if constexpr (I >= 14 && I < 22 && !(I & 1)) W2P((I - 14) >> 1, sA, lA);
      else if constexpr (I >= 22 && I < 30 && !(I & 1)) W2P((I - 22) >> 1, sB, lB);
    );
  }
  bar_vm<8>();
  {
    const u32 a = ST_OFF + (lane & 31) * 4;
    mnA = lds32f(a); mnB = lds32f(a + 128); k1A = lds32f(a + 256); k1B = lds32f(a + 384); irA = lds32f(a + 512); irB = lds32f(a + 640);
  }
  EPI(mnA, k1A, irA);
  ST_F(0);
  const v4u fa0 = f0_, fa1 = f1_;
  P3_PRE(1);
  bar_vm<2>();
  {
    u32 sA = th ? W2SRC(1, 3) : W2SRC(1, 2), sB = th ? W2SRC(0, 4) : W2SRC(0, 3), lA = (th ? 0 : 5 * UNIT) + ld4k, lB = lA + UNIT;
    asm volatile("" : "+s"(sA), "+s"(sB), "+s"(lA), "+s"(lB));
    P3_LOOP(1,
      if constexpr (I >= 4 && I < 28 && !(I & 1)) RD_W2F((I - 4) >> 3, ((I - 4) >> 1) & 3, ((I - 4) >> 3) * UNIT);
      if constexpr (I < 8 && (I & 1)) W2P(I >> 1, sA, lA);
      else if constexpr (I >= 8 && I < 16 && (I & 1)) W2P((I - 8) >> 1, sB, lB);
    );
  }
  bar_vm<0>();
  EPI(mnB, k1B, irB);
  ST_F(112 * 1024u);
  RD_W2(3, w2d);
  if (!th) {
    DMA_W2(0, 5, 2);
    bar();
    if (w == 0) put_flag<384>(rsW, fla, flt); else __builtin_amdgcn_s_sleep(1);
    DMA_W2(0, 6, 3); DMA_W2(1, 4, 4);
  }
  bar();
  if (!th) { DMA_W2(1, 5, 5); DMA_W2(1, 6, 6); }
  if (w == 3) poll_flags(rsW, ctl + 384, 7, tag, lane);
  bar_vm<0>();
  if (w == 0) put_flag<512>(rsW, fla, flt); else __builtin_amdgcn_s_sleep(1);
  v4u ff[14];
  const u32 fro = opq(lin16 + F_OFF + (u32)(tt * 2 * 112 + 2 * w) * 1024u);
#define LD_F(a, i) ff[i] = __builtin_amdgcn_raw_buffer_load_b128(rsW, fro + ((i) & 1) * 1024u, ((a) * 112 + 16 * PC((i) >> 1)) * 1024u, 16)
  v2u fl;
  const u32 fpa = opq(lane < 7 ? ctl + 512 + lane * 8 : 0x40000000u);
  ff[0] = fa0; ff[1] = fa1;
  if (ct == 7) { LD_F(0, 0); LD_F(0, 1); }
  sfor<2, 6>([&](auto ii) { LD_F(0, decltype(ii)::value); SB(); });
  SB();
  RD_W2(4, UNIT); RD_W2(5, 2 * UNIT); RD_W2(6, 3 * UNIT);
  SB();
  sfor<6, RD_>([&](auto ii) { LD_F(0, decltype(ii)::value); SB(); });
  SB();
  v16f o0, o1;
  const u32 rwa = opq(h * 4096 + (lane & 15) * 256 + ((hf ^ (lane & 7)) * 16));
  u32 rw[4];
  auto red_addr = [&](u32 base) {
#pragma unroll
    for (int a4 = 0; a4 < 4; ++a4) { rw[a4] = (rwa ^ (a4 * 32)) + base; PIN(rw[a4]); }
  };
  auto red_write = [&]() {
#pragma unroll
    for (int a4 = 0; a4 < 4; ++a4) {
      *(LAS v4f*)(rw[a4]) = v4f{o0[4 * a4], o0[4 * a4 + 1], o0[4 * a4 + 2], o0[4 * a4 + 3]};
      *(LAS v4f*)(rw[a4] + 128) = v4f{o1[4 * a4], o1[4 * a4 + 1], o1[4 * a4 + 2], o1[4 * a4 + 3]};
    }
  };
  red_addr((1 + 3 * th + blk) * UNIT);
#define P4_HALF(a, ...) sfor<0, 14>([&](auto ii) { constexpr int I = decltype(ii)::value; \
    if constexpr (I + RD_ < 14) { LD_F(a, I + RD_); SB(); } \
    if constexpr (I == 0) { v16f z_; for (int e = 0; e < 16; ++e) z_[e] = 0.f; o0 = mfma32(w2f[0], ff[0], z_); o1 = mfma32(w2f[1], ff[0], z_); } \
    else { o0 = mfma32(w2f[2 * I], ff[I], o0); o1 = mfma32(w2f[2 * I + 1], ff[I], o1); } \
    PIN(o0); PIN(o1); \
    __VA_ARGS__ \
    SB(); })
  P4_HALF(0, if constexpr (I == 14 - RD_) fl = __builtin_amdgcn_raw_buffer_load_b64(rsW, fpa, 0, 16););
  if ((__builtin_amdgcn_ballot_w64(fl.x == (u32)tag && fl.y == (u32)(tag >> 32)) & 0x7f) != 0x7f) poll_flags<4>(rsW, ctl + 512, 7, tag, lane);
  asm volatile("" ::: "memory");
  ff[0] = f0_; ff[1] = f1_;
  if (ct == 7) { LD_F(1, 0); LD_F(1, 1); }
  sfor<2, 6>([&](auto ii) { LD_F(1, decltype(ii)::value); SB(); });
  SB();
  red_write();
  red_addr(th ? (blk ? 0 : 6 * UNIT) : (7 + blk) * UNIT);
  SB();
  sfor<6, RD_>([&](auto ii) { LD_F(1, decltype(ii)::value); SB(); });
  SB();
  const __amdgpu_buffer_rsrc_t rsO = __builtin_amdgcn_make_buffer_rsrc(out, 0, SEQ * DM * 2, 0x00020000);
  const int tk = (tid >> 4) & 15, c4 = tid & 15;
  const u32 ha = HRES_OFF + (c4 >> 2) * 1024 + (((c4 >> 1) & 1) * 32 + tk) * 16 + (c4 & 1) * 8;
  const u32 rb0 = tk * 256 + ((c4 ^ (tk & 7)) * 16);
  u32 ax = rb0 + UNIT, ay = rb0 + 4 * UNIT, bx = rb0 + (7 + th) * UNIT, by = rb0 + (th ? 0 : 6 * UNIT);
  u32 oo = (u32)(t0 + tk) * 1024u + (u32)c * 128u + c4 * 8u, ob = oo + 32768u + th * 16384u;
  const v2u hr0 = lds64(ha), hr1 = lds64(ha + 256), hr2 = lds64(ha + 4096 + th * 256);
  PIN(ax); PIN(ay); PIN(bx); PIN(by); PIN(oo); PIN(ob);
  auto sum8 = [&](const v4f* t, v2u hr) {
    const v4f r = (((t[0] + t[1]) + (t[2] + t[3])) + ((t[4] + t[5]) + (t[6] + t[7]))) + v4f{bflo(hr.x), bfhi(hr.x), bflo(hr.y), bfhi(hr.y)};
    return v2u{pk(r.x, r.y), pk(r.z, r.w)};
  };
  P4_HALF(1, );
  if (th) bar();
  red_write();
  if (!th) {
    bar();
    v4f pt[16];
#pragma unroll
    for (int r = 0; r < 2; ++r)
#pragma unroll
      for (int i = 0; i < 4; ++i) { pt[8 * r + i] = lds128f(ax + r * UNIT + i * 4096); pt[8 * r + 4 + i] = lds128f(ay + r * UNIT + i * 4096); }
    SB();
    __builtin_amdgcn_raw_buffer_store_b64(sum8(pt, hr0), rsO, oo, 0, 0);
    __builtin_amdgcn_raw_buffer_store_b64(sum8(pt + 8, hr1), rsO, oo, 16384, 0);
  }
  bar();
  asm volatile(".Lfb_end:");
  {
    v4f pt[8];
#pragma unroll
    for (int i = 0; i < 4; ++i) { pt[i] = lds128f(bx + i * 4096); pt[4 + i] = lds128f(by + i * 4096); }
    SB();
    __builtin_amdgcn_raw_buffer_store_b64(sum8(pt, hr2), rsO, ob, 0, 0);
  }
}

extern "C" void kernel_launch(void* const* d_in, const int* in_sizes, int n_in, void* d_out, int out_size, void* d_ws, size_t ws_size,
                              hipStream_t stream) {
  (void)hipFuncSetAttribute((const void*)fused_block, hipFuncAttributeMaxDynamicSharedMemorySize, LDS_BYTES);
  fused_block<<<256, 512, LDS_BYTES, stream>>>(d_in[0], d_in[1], d_in[2], d_in[4], d_in[5], d_out, d_ws);
}
```
